```python
import torch

def _softmax(x, axis=-1):
    return torch.softmax(x, dim=axis)
def _gelu(x):
    return torch.nn.functional.gelu(x, approximate="tanh")
def _silu(x):
    return torch.nn.functional.silu(x)
def _sigmoid(x):
    return torch.sigmoid(x)
def _layernorm(x, axis=-1, eps=1e-5):
    m = x.mean(dim=axis, keepdim=True)
    v = x.var(dim=axis, keepdim=True, unbiased=False)
    return (x - m) / torch.sqrt(v + eps)
def _scatter_add(tgt, idx, src, axis=0):
    return tgt.clone().index_add_(axis, idx, src)
def _segment_sum(src, seg, n):
    out = torch.zeros((n,) + src.shape[1:], dtype=src.dtype)
    return out.index_add_(0, seg, src)
def _pad(x, pw):
    flat = tuple(v for pair in reversed(pw) for v in pair)
    return torch.nn.functional.pad(x, flat)

kernel_name = 'gated_mix__linear_kernel_hedgehog__linear_kernel_hedgehog__prompt'

def setup_inputs():

    _w = (lambda a: (lambda t: t.to(torch.bfloat16) if t.is_floating_point() else t)(torch.as_tensor(a)))
    def _A_setup():
        B, S, D, H = 1, 2048, 448, 16
        Dh = D // H
        return {
            "x": _w((lambda *s: torch.randn(*s, dtype=torch.bfloat16) * 0.05)(B, S, D)),
            "wqkv": _w((lambda *s: torch.randn(*s, dtype=torch.bfloat16) * 0.8)(D, 3 * D)),
            "w_phi": _w((lambda *s: torch.randn(*s, dtype=torch.bfloat16) * 0.8)(H, Dh, Dh) * (Dh ** -0.5)),
            "wo": _w((lambda *s: torch.randn(*s, dtype=torch.bfloat16) * 0.05)(D, D)),
        }
    _A_in = _A_setup()

    def _B_setup():
        B, S, D, H = 1, 2048, 448, 16
        Dh = D // H
        return {
            "x": _w((lambda *s: torch.randn(*s, dtype=torch.bfloat16) * 0.05)(B, S, D)),
            "wqkv": _w((lambda *s: torch.randn(*s, dtype=torch.bfloat16) * 0.8)(D, 3 * D)),
            "w_phi": _w((lambda *s: torch.randn(*s, dtype=torch.bfloat16) * 0.8)(H, Dh, Dh) * (Dh ** -0.5)),
            "wo": _w((lambda *s: torch.randn(*s, dtype=torch.bfloat16) * 0.05)(D, D)),
        }
    _B_in = _B_setup()

    return {
        "A_x": _A_in["x"],
        "A_wqkv": _A_in["wqkv"],
        "A_w_phi": _A_in["w_phi"],
        "A_wo": _A_in["wo"],
        "B_x": _B_in["x"],
        "B_wqkv": _B_in["wqkv"],
        "B_w_phi": _B_in["w_phi"],
        "B_wo": _B_in["wo"],
        "gate_w": _w((lambda *s: torch.randn(*s, dtype=torch.bfloat16) * 0.05)(448) * 16.0),
    }

def reference(A_x, A_wqkv, A_w_phi, A_wo, B_x, B_wqkv, B_w_phi, B_wo, gate_w):
    def _A_ref(x, wqkv, w_phi, wo):
        B, S, D = x.shape
        H = 16; Dh = D // H
        qkv = (lambda x, *d: x.permute(*d))((x @ wqkv).reshape(B, S, 3, H, Dh), 2, 0, 3, 1, 4)
        q, k, v = qkv[0], qkv[1], qkv[2]
        pq = torch.einsum("bhsd,hdf->bhsf", q, w_phi)
        pk = torch.einsum("bhsd,hdf->bhsf", k, w_phi)
        phi_q = torch.concatenate([_softmax(pq, axis=-1), _softmax(-pq, axis=-1)], axis=-1)
        phi_k = torch.concatenate([_softmax(pk, axis=-1), _softmax(-pk, axis=-1)], axis=-1)
        kv = torch.einsum("bhsf,bhsd->bhfd", phi_k, v)
        z  = torch.einsum("bhsf->bhf", phi_k)
        num = torch.einsum("bhqf,bhfd->bhqd", phi_q, kv)
        den = torch.einsum("bhqf,bhf->bhq", phi_q, z)[..., None] + 1e-6
        o = num / den
        return (lambda x, *d: x.permute(*d))(o, 0, 2, 1, 3).reshape(B, S, D) @ wo

    def _B_ref(x, wqkv, w_phi, wo):
        B, S, D = x.shape
        H = 16; Dh = D // H
        qkv = (lambda x, *d: x.permute(*d))((x @ wqkv).reshape(B, S, 3, H, Dh), 2, 0, 3, 1, 4)
        q, k, v = qkv[0], qkv[1], qkv[2]
        pq = torch.einsum("bhsd,hdf->bhsf", q, w_phi)
        pk = torch.einsum("bhsd,hdf->bhsf", k, w_phi)
        phi_q = torch.concatenate([_softmax(pq, axis=-1), _softmax(-pq, axis=-1)], axis=-1)
        phi_k = torch.concatenate([_softmax(pk, axis=-1), _softmax(-pk, axis=-1)], axis=-1)
        kv = torch.einsum("bhsf,bhsd->bhfd", phi_k, v)
        z  = torch.einsum("bhsf->bhf", phi_k)
        num = torch.einsum("bhqf,bhfd->bhqd", phi_q, kv)
        den = torch.einsum("bhqf,bhf->bhq", phi_q, z)[..., None] + 1e-6
        o = num / den
        return (lambda x, *d: x.permute(*d))(o, 0, 2, 1, 3).reshape(B, S, D) @ wo

    x = A_x
    g = _sigmoid((x * gate_w).sum(-1, keepdims=True))
    a = _A_ref(x, A_wqkv, A_w_phi, A_wo)
    b = _B_ref(x, B_wqkv, B_w_phi, B_wo)
    return g * a + (1 - g) * b
```

```cpp
#include <hip/hip_runtime.h>
#include <hip/hip_bf16.h>
typedef __hip_bfloat16 bf16;
constexpr int S=2048, D=448, H=16, DH=28;
__device__ inline float b2f(bf16 v){return __bfloat162float(v);}
__device__ inline float rb(float v){return __bfloat162float(__float2bfloat16(v));}
__global__ void k_qkv(const bf16* x, const bf16* wA, const bf16* wB, float* qkv){
  int n = blockIdx.x*64 + (threadIdx.x&63); int s0 = blockIdx.y*16 + (threadIdx.x>>6)*4; int b = blockIdx.z;
  const bf16* w = b? wB: wA;
  float a[4]={0,0,0,0};
  for(int k=0;k<D;k++){ float wv=b2f(w[k*1344+n]); for(int i=0;i<4;i++) a[i]+=b2f(x[(s0+i)*D+k])*wv; }
  for(int i=0;i<4;i++) qkv[((size_t)b*S+s0+i)*1344+n]=rb(a[i]);
}
__device__ __forceinline__ void phi(const float* q, const bf16* wp, float* out){
  float p[DH]; float mx=-1e30f, mn=1e30f;
  _Pragma("unroll") for(int f=0;f<DH;f++){ float a=0; _Pragma("unroll") for(int d=0;d<DH;d++) a+=q[d]*b2f(wp[d*DH+f]); a=rb(a); p[f]=a; mx=fmaxf(mx,a); mn=fminf(mn,a);}
  float s1=0,s2=0;
  _Pragma("unroll") for(int f=0;f<DH;f++){ out[f]=__expf(p[f]-mx); out[DH+f]=__expf(mn-p[f]); s1+=out[f]; s2+=out[DH+f];}
  _Pragma("unroll") for(int f=0;f<DH;f++){ out[f]=rb(out[f]/s1); out[DH+f]=rb(out[DH+f]/s2);}
}
__global__ void __launch_bounds__(128) k_kv(const float* qkv, const bf16* wpA, const bf16* wpB, float* part){
  __shared__ float ph[128][57]; __shared__ float vv[128][29];
  int bh=blockIdx.x, b=bh>>4, h=bh&15; int s=blockIdx.y*128+threadIdx.x;
  const bf16* wp=(b?wpB:wpA)+h*DH*DH;
  const float* row=qkv+((size_t)b*S+s)*1344;
  float k[DH]; _Pragma("unroll") for(int d=0;d<DH;d++) k[d]=row[448+h*DH+d];
  float o[56]; phi(k,wp,o);
  _Pragma("unroll") for(int f=0;f<56;f++) ph[threadIdx.x][f]=o[f];
  for(int d=0;d<DH;d++) vv[threadIdx.x][d]=row[896+h*DH+d];
  vv[threadIdx.x][28]=1.f;
  __syncthreads();
  for(int i=threadIdx.x;i<56*29;i+=128){ int f=i/29,d=i%29; float a=0; for(int r=0;r<128;r++) a+=ph[r][f]*vv[r][d]; part[((size_t)bh*16+blockIdx.y)*56*29+i]=a; }
}
__global__ void __launch_bounds__(128) k_o(const float* qkv, const bf16* wpA, const bf16* wpB, const float* part, float* o){
  __shared__ float kv[56*29];
  int bh=blockIdx.x, b=bh>>4, h=bh&15; int s=blockIdx.y*128+threadIdx.x;
  for(int i=threadIdx.x;i<56*29;i+=128){ float a=0; for(int r=0;r<16;r++) a+=part[((size_t)bh*16+r)*56*29+i]; kv[i]=rb(a);}
  __syncthreads();
  const bf16* wp=(b?wpB:wpA)+h*DH*DH;
  const float* row=qkv+((size_t)b*S+s)*1344;
  float q[DH]; _Pragma("unroll") for(int d=0;d<DH;d++) q[d]=row[h*DH+d];
  float p[56]; phi(q,wp,p);
  float den=0; _Pragma("unroll") for(int f=0;f<56;f++) den+=p[f]*kv[f*29+28];
  den=rb(rb(den)+1e-6f);
  for(int d=0;d<DH;d++){ float a=0; _Pragma("unroll") for(int f=0;f<56;f++) a+=p[f]*kv[f*29+d]; o[((size_t)b*S+s)*D+h*DH+d]=rb(rb(a)/den);}
}
__global__ void k_out(const bf16* x, const bf16* gw, const float* o, const bf16* woA, const bf16* woB, bf16* out){
  int s=blockIdx.x; __shared__ float red[448]; __shared__ float oa[448], ob[448];
  int t=threadIdx.x;
  red[t]=rb(b2f(x[s*D+t])*b2f(gw[t])); oa[t]=o[(size_t)s*D+t]; ob[t]=o[((size_t)S+s)*D+t];
  __syncthreads();
  float g=0; for(int i=0;i<448;i++) g+=red[i]; g=rb(g); g=rb(1.f/(1.f+__expf(-g)));
  float a=0,b=0; for(int k=0;k<D;k++){ a+=oa[k]*b2f(woA[k*D+t]); b+=ob[k]*b2f(woB[k*D+t]); }
  a=rb(a); b=rb(b);
  out[s*D+t]=__float2bfloat16(rb(g*a)+rb(rb(1.f-g)*b));
}
__global__ void k_empty(){}
#define NEMPTY 0
extern "C" void kernel_launch(void* const* d_in, const int* in_sizes, int n_in,
                              void* d_out, int out_size, void* d_ws, size_t ws_size,
                              hipStream_t stream) {
  const bf16* x=(const bf16*)d_in[0]; const bf16* wqA=(const bf16*)d_in[1]; const bf16* wpA=(const bf16*)d_in[2]; const bf16* woA=(const bf16*)d_in[3];
  const bf16* wqB=(const bf16*)d_in[5]; const bf16* wpB=(const bf16*)d_in[6]; const bf16* woB=(const bf16*)d_in[7]; const bf16* gw=(const bf16*)d_in[8];
  float* qkv=(float*)d_ws; float* part=qkv+(size_t)2*S*1344; float* o=part+(size_t)32*16*56*29;
  k_qkv<<<dim3(21,128,2),256,0,stream>>>(x,wqA,wqB,qkv);
  k_kv<<<dim3(32,16),128,0,stream>>>(qkv,wpA,wpB,part);
  k_o<<<dim3(32,16),128,0,stream>>>(qkv,wpA,wpB,part,o);
  k_out<<<2048,448,0,stream>>>(x,gw,o,woA,woB,(bf16*)d_out);
  for(int i=0;i<NEMPTY;i++) k_empty<<<256,64,0,stream>>>();
}
```

```cpp
#include <hip/hip_runtime.h>
#include <hip/hip_bf16.h>
#include <stdint.h>

#define STAMPS 0
#define MAP1 1

typedef __attribute__((ext_vector_type(8))) __bf16 bf16x8;
typedef __attribute__((ext_vector_type(2))) __bf16 bf16x2;
typedef __attribute__((ext_vector_type(4))) short s16x4;
typedef __attribute__((ext_vector_type(16))) float f32x16;
typedef __attribute__((ext_vector_type(4))) float f32x4;
typedef __attribute__((ext_vector_type(4))) unsigned u32x4;
typedef __attribute__((ext_vector_type(3))) unsigned u32x3;
typedef __attribute__((ext_vector_type(2))) unsigned u32x2;
typedef unsigned long long u64;
#define LAS __attribute__((address_space(3)))
#define GAS __attribute__((address_space(1)))
#define DI __device__ __forceinline__
#define RLX_AGENT __ATOMIC_RELAXED, __HIP_MEMORY_SCOPE_AGENT

constexpr int SEQ = 2048, DM = 448, DH = 28;
constexpr int XROW = DM * 2;
constexpr int WROW = 3 * DM * 2;
constexpr int AUX_SC1 = 16, RSRC3 = 0x00020000;

constexpr int L_WKV = 0;
constexpr int L_XS0 = 57344;
constexpr int L_XS1 = L_XS0 + 32768;
constexpr int L_XS2 = L_XS1 + 32768;
constexpr int L_WPHI = 155648;
constexpr int L_GATE = L_WPHI + 2048;
constexpr int L_E = L_GATE + 1024;
constexpr int L_KVT = L_WPHI + 4096;
constexpr int LDS_BYTES = 163840;

constexpr int WS_E = 0;
constexpr int WS_FA1 = 4096;
constexpr int WS_FA2 = WS_FA1 + 16384;
constexpr int WS_PART = 65536;
constexpr int WS_O = WS_PART + 32 * 64 * 1024;
constexpr int OBLK = 1792;
constexpr int WS_END = WS_O + 64 * 32 * OBLK;
constexpr int WS_STAMP = 8 << 20;
constexpr unsigned SPIN_CAP = 1u << 14;
constexpr unsigned EMPTY = 0xFFFFFFFFu;
#define WPC 1
#define FA2STEP 20
#define NOX 0
#define NOW 0
#define PBAR 0
#define NOCLR 0
#define CHST 0
#define DELAY2 0

struct Params {
    const void* x; const void* wqkv0; const void* wqkv1; const void* wphi0; const void* wphi1;
    const void* wo0; const void* wo1; const void* gate; void* out; void* ws;
};

DI unsigned pk2(float a, float b) { bf16x2 v = {(__bf16)a, (__bf16)b}; return __builtin_bit_cast(unsigned, v); }
DI f32x16 mfma32(u32x4 a, u32x4 b, f32x16 c) {
    return __builtin_amdgcn_mfma_f32_32x32x16_bf16(__builtin_bit_cast(bf16x8, a), __builtin_bit_cast(bf16x8, b), c, 0, 0, 0);
}
DI f32x4 mfma16(u32x4 a, u32x4 b, f32x4 c) {
    return __builtin_amdgcn_mfma_f32_16x16x32_bf16(__builtin_bit_cast(bf16x8, a), __builtin_bit_cast(bf16x8, b), c, 0, 0, 0);
}
DI u32x2 trd(unsigned a) { return __builtin_bit_cast(u32x2, __builtin_amdgcn_ds_read_tr16_b64_v4i16((LAS s16x4*)a)); }
DI u32x4 trfrag(unsigned a) { u32x2 lo = trd(a), hi = trd(a + 256); return u32x4{lo.x, lo.y, hi.x, hi.y}; }
DI u32x4 ld128(unsigned a) { return *(LAS const u32x4*)a; }
DI void st128(unsigned a, u32x4 v) { *(LAS u32x4*)a = v; }
DI void st64(unsigned a, u32x2 v) { *(LAS u32x2*)a = v; }
template <int OFF> DI void dma16(unsigned m0, unsigned voff, __amdgpu_buffer_rsrc_t srd, unsigned soff) {
    asm volatile("s_mov_b32 m0, %0\n\ts_nop 0\n\tbuffer_load_dwordx4 %1, %2, %3 offen offset:%4 lds"
                 :: "s"(m0), "v"(voff), "s"(srd), "s"(soff), "i"(OFF) : "memory");
}
DI u32x4 gld16(unsigned voff, __amdgpu_buffer_rsrc_t srd, unsigned soff) {
    u32x4 r; asm volatile("buffer_load_dwordx4 %0, %1, %2, %3 offen" : "=v"(r) : "v"(voff), "s"(srd), "s"(soff) : "memory"); return r;
}
DI void dma4(unsigned m0, unsigned voff, __amdgpu_buffer_rsrc_t srd) {
    asm volatile("s_mov_b32 m0, %0\n\ts_nop 0\n\tbuffer_load_dword %1, %2, 0 offen sc1 lds" :: "s"(m0), "v"(voff), "s"(srd) : "memory");
}
template <int N> DI void wait_vm() { asm volatile("s_waitcnt vmcnt(%0)" :: "i"(N) : "memory"); }
DI void barrier() { asm volatile("s_waitcnt lgkmcnt(0)\n\ts_barrier" ::: "memory"); }
DI float xmax(float v) { auto r = __builtin_amdgcn_permlane32_swap(__float_as_uint(v), __float_as_uint(v), false, false); return __builtin_fmaxf(__uint_as_float(r[0]), __uint_as_float(r[1])); }
DI float xmin(float v) { auto r = __builtin_amdgcn_permlane32_swap(__float_as_uint(v), __float_as_uint(v), false, false); return __builtin_fminf(__uint_as_float(r[0]), __uint_as_float(r[1])); }
DI float xsum(float v) { auto r = __builtin_amdgcn_permlane32_swap(__float_as_uint(v), __float_as_uint(v), false, false); return __uint_as_float(r[0]) + __uint_as_float(r[1]); }
DI float xupper(float v) { auto r = __builtin_amdgcn_permlane32_swap(__float_as_uint(v), __float_as_uint(v), false, false); return __uint_as_float(r[1]); }

#define STAMP(i) do {} while (0)

#define STAMPX(i) STAMP(i)
DI void hedgehog(const f32x16& p, u32x4& pos0, u32x4& pos1, u32x4& neg0, u32x4& neg1) {
    constexpr float C = 1.4426950408889634f;
    float mx = __builtin_fmaxf(p[0], p[1]), mn = __builtin_fminf(p[0], p[1]);
#pragma unroll
    for (int i = 2; i < 14; i += 2) { mx = __builtin_fmaxf(__builtin_fmaxf(mx, p[i]), p[i + 1]); mn = __builtin_fminf(__builtin_fminf(mn, p[i]), p[i + 1]); }
    mx = xmax(mx); mn = xmin(mn);
    const float a = -mx * C, b = mn * C;
    float ep[14], en[14];
#pragma unroll
    for (int i = 0; i < 14; ++i) { ep[i] = __builtin_amdgcn_exp2f(__builtin_fmaf(p[i], C, a)); en[i] = __builtin_amdgcn_exp2f(__builtin_fmaf(p[i], -C, b)); }
    float sp = 0.f, sn = 0.f;
#pragma unroll
    for (int i = 0; i < 14; ++i) { sp += ep[i]; sn += en[i]; }
    const float rp = __builtin_amdgcn_rcpf(xsum(sp)), rn = __builtin_amdgcn_rcpf(xsum(sn));
    pos0 = u32x4{pk2(ep[0] * rp, ep[1] * rp), pk2(ep[2] * rp, ep[3] * rp), pk2(ep[4] * rp, ep[5] * rp), pk2(ep[6] * rp, ep[7] * rp)};
    pos1 = u32x4{pk2(ep[8] * rp, ep[9] * rp), pk2(ep[10] * rp, ep[11] * rp), pk2(ep[12] * rp, ep[13] * rp), 0u};
    neg0 = u32x4{pk2(en[0] * rn, en[1] * rn), pk2(en[2] * rn, en[3] * rn), pk2(en[4] * rn, en[5] * rn), pk2(en[6] * rn, en[7] * rn)};
    neg1 = u32x4{pk2(en[8] * rn, en[9] * rn), pk2(en[10] * rn, en[11] * rn), pk2(en[12] * rn, en[13] * rn), 0u};
}
DI u32x4 pack8(const f32x16& a, int s) { return u32x4{pk2(a[8 * s], a[8 * s + 1]), pk2(a[8 * s + 2], a[8 * s + 3]), pk2(a[8 * s + 4], a[8 * s + 5]), pk2(a[8 * s + 6], a[8 * s + 7])}; }

DI unsigned nz(unsigned v) { return v < EMPTY - 1 ? v : EMPTY - 1; }
DI unsigned umax3(unsigned a, unsigned b, unsigned c) { unsigned m = a > b ? a : b; return m > c ? m : c; }
DI unsigned umax4(unsigned m, u32x4 v) { return umax3(umax3(m, v.x, v.y), v.z, v.w); }
#define SB() __builtin_amdgcn_sched_barrier(0)

__global__ void __launch_bounds__(512, 2) hedgehog_mix_kernel(Params P) {
    STAMP(0);
    const int tid = threadIdx.x, l = tid & 63;
    const int w = __builtin_amdgcn_readfirstlane(tid >> 6);
    const int bid = blockIdx.x, xcd = bid & 7, idx = bid >> 3;
    const int rb = idx >> 2, bh = xcd * 4 + (idx & 3);
    const int br = bh >> 4, hd = bh & 15;
    const bool has3 = idx < 28;
    const int rt3 = xcd * 4 + (idx & 3), ct3 = idx >> 2;
    const int r32 = l & 31, hh = l >> 5;

    const __amdgpu_buffer_rsrc_t rs_x = __builtin_amdgcn_make_buffer_rsrc((void*)P.x, 0, NOX ? 0 : SEQ * XROW, RSRC3);
    const __amdgpu_buffer_rsrc_t rs_w = __builtin_amdgcn_make_buffer_rsrc((void*)(br ? P.wqkv1 : P.wqkv0), 0, NOW ? 0 : DM * WROW, RSRC3);
    const __amdgpu_buffer_rsrc_t rs_ws = __builtin_amdgcn_make_buffer_rsrc(P.ws, 0, WS_END, RSRC3);

    const unsigned vw = (l >> 2) * WROW + (l & 3) * 16;
    const unsigned xrow0 = (rb * 256 + w * 32) * XROW;
    const unsigned vxe = (l >> 3) * XROW + (((l & 7) ^ (l >> 4)) * 16), vxo = (l >> 3) * XROW + (((l & 7) ^ (4 + (l >> 4))) * 16);
    const unsigned wso = (w & 3) * 16 * WROW + (1 + (w >> 2)) * XROW + hd * 56, wm0 = L_WKV + (w >> 2) * 28672 + (w & 3) * 1024;
    auto load_w = [&](int c) { return gld16(vw, rs_w, wso + c * 64 * WROW); };
    const unsigned wst = wm0 + l * 16;
    auto issue_x = [&](int c) {
        const unsigned m0 = L_XS0 + (c % 3) * 32768 + w * 4096, so = xrow0 + c * 128;
        dma16<0>(m0, vxe, rs_x, so);
        dma16<1024>(m0, vxo, rs_x, so + 8 * XROW - 1024);
        dma16<2048>(m0, vxe, rs_x, so + 16 * XROW - 2048);
        dma16<3072>(m0, vxo, rs_x, so + 24 * XROW - 3072);
    };
    asm volatile("s_nop 4" ::: "memory");
    if (w == 0) {
        dma4(L_E, (l & 1) * 4, rs_ws);
    } else if (w == 1) {
        const __amdgpu_buffer_rsrc_t rs_g = __builtin_amdgcn_make_buffer_rsrc((void*)P.gate, 0, XROW, RSRC3);
        dma16<0>(L_GATE, l * 16, rs_g, 0);
    } else if (w < 4) {
        const __amdgpu_buffer_rsrc_t rs_p = __builtin_amdgcn_make_buffer_rsrc((void*)(br ? P.wphi1 : P.wphi0), 0, 16 * DH * DH * 2, RSRC3);
        dma16<0>(L_WPHI + (w - 2) * 1024, l * 16, rs_p, hd * (DH * DH * 2) + (w - 2) * 1024);
    }
    u32x4 wa0 = load_w(0), wa1 = load_w(1), wa2 = load_w(2);
    issue_x(0); issue_x(1);
    const int part_off = WS_PART + ((bh * 8 + w) * 8 + rb) * 1024 + l * 16;
    const int o_off = WS_O + ((rb * 8 + w) * 32 + bh) * OBLK;
    const u32x4 e4 = {EMPTY, EMPTY, EMPTY, EMPTY};
    if (!NOCLR) asm volatile("buffer_store_dwordx4 %0, %1, %2, 0 offen sc1\n\ts_nop 1" :: "v"(e4), "v"(part_off), "s"(rs_ws) : "memory");
    STAMP(1);

    const unsigned trl = (8 * hh + ((l & 15) >> 2)) * 64 + ((l >> 4) & 1) * 32 + (l & 3) * 8;
    unsigned xa[4];
#pragma unroll
    for (int t = 0; t < 4; ++t) xa[t] = L_XS0 + w * 4096 + r32 * 128 + (((hh ^ ((r32 >> 1) & 7)) * 16) ^ (32 * t));
    u32x4 xf[28];
    u32x4 wp0, wp1;
    u64 tag = 0;
    f32x16 ak = {}, av = {};
    constexpr int CL = NOCLR ? 0 : 1;
    u32x4 wb3, wb4, wb5, wb6, wq0, wq1, wq2, wq3;
    asm volatile("s_waitcnt vmcnt(%3)" : "+v"(wa0), "+v"(wa1), "+v"(wa2) : "i"(8 + CL) : "memory");
    st128(wst, wa0); st128(wst + 4096, wa1); st128(wst + 8192, wa2);
#pragma unroll
    for (int c = 0; c < 7; ++c) {
        if (c == 0) wait_vm<4 + CL>(); else if (c == 1) wait_vm<8 + CL>(); else if (c == 3) wait_vm<5>(); else wait_vm<4>();
        if (c == 2) {
            asm volatile("" : "+v"(wb3), "+v"(wb4), "+v"(wb5), "+v"(wb6));
            st128(wst + 3 * 4096, wb3); st128(wst + 4 * 4096, wb4); st128(wst + 5 * 4096, wb5); st128(wst + 6 * 4096, wb6);
        }
        if (c == 0 || c == 3) barrier();
        if (c == 0) STAMP(2);
        if (c == 2 && l == 0) __hip_atomic_store((GAS u64*)((GAS char*)P.ws + WS_FA1) + (bh * 8 + w) * 8 + rb, tag, RLX_AGENT);
#pragma unroll
        for (int t = 0; t < 4; ++t) xf[4 * c + t] = ld128(xa[t] + (c % 3) * 32768);
        u32x4 fk[2], fv[2];
        fk[0] = trfrag(L_WKV + trl + 4 * c * 1024); fv[0] = trfrag(L_WKV + 28672 + trl + 4 * c * 1024);
        SB();
        asm volatile("" : "+v"(xf[4 * c]), "+v"(xf[4 * c + 1]), "+v"(xf[4 * c + 2]), "+v"(xf[4 * c + 3]));
        if (c == 0) { wb3 = load_w(3); wb4 = load_w(4); wb5 = load_w(5); wb6 = load_w(6); }
        if (c + 2 <= 6) issue_x(c + 2);
        else if (c == 5) {
            const unsigned so = (w < 7 ? w : 6) * 64 * WROW + hd * 56;
            wq0 = gld16(vw, rs_w, so); wq1 = gld16(vw, rs_w, so + 16 * WROW); wq2 = gld16(vw, rs_w, so + 32 * WROW); wq3 = gld16(vw, rs_w, so + 48 * WROW);
        }
        if (c == 0) tag = *(LAS const u64*)L_E + 1;
        if (c == WPC) {
            const int hp = (r32 >> 2) & 1, rg = (r32 & 3) + 4 * (r32 >> 3);
            const bool fvalid = rg < 14;
            const int f = fvalid ? 14 * hp + rg : 0;
            unsigned short e[16];
#pragma unroll
            for (int s = 0; s < 2; ++s)
#pragma unroll
                for (int j = 0; j < 8; ++j) {
                    const int d = 16 * s + 8 * (j >> 2) + 4 * hh + (j & 3);
                    const bool ok = fvalid && d < DH;
                    const unsigned short v = *(LAS const unsigned short*)(L_WPHI + ((ok ? d : 0) * DH + f) * 2);
                    e[8 * s + j] = ok ? v : (unsigned short)0;
                }
            wp0 = u32x4{e[0] | (unsigned)e[1] << 16, e[2] | (unsigned)e[3] << 16, e[4] | (unsigned)e[5] << 16, e[6] | (unsigned)e[7] << 16};
            wp1 = u32x4{e[8] | (unsigned)e[9] << 16, e[10] | (unsigned)e[11] << 16, e[12] | (unsigned)e[13] << 16, e[14] | (unsigned)e[15] << 16};
        }
        SB();
#pragma unroll
        for (int t = 0; t < 4; ++t) {
            const int ks = 4 * c + t;
            if (t < 3) { fk[(t + 1) & 1] = trfrag(L_WKV + trl + (ks + 1) * 1024); fv[(t + 1) & 1] = trfrag(L_WKV + 28672 + trl + (ks + 1) * 1024); }
            SB();
            ak = mfma32(fk[t & 1], xf[ks], ak);
            av = mfma32(xf[ks], fv[t & 1], av);
            SB();
        }
    }
    STAMP(3);
    const GAS u64* fap = (const GAS u64*)((GAS char*)P.ws + WS_FA1) + (bh * 8 + w) * 8 + (l & 7);
    u64 faw = __hip_atomic_load(fap, RLX_AGENT);
    {
        f32x16 p = {};
        p = mfma32(wp0, pack8(ak, 0), p);
        p = mfma32(wp1, pack8(ak, 1), p);
        const int ph = (0x78 >> (2 * ((r32 >> 2) & 3))) & 3;
        const unsigned va = L_XS1 + w * 4096 + r32 * 64 + 8 * hh;
        const bool one = r32 == 28;
#pragma unroll
        for (int g = 0; g < 4; ++g) {
            u32x2 v2 = {pk2(av[4 * g], av[4 * g + 1]), pk2(av[4 * g + 2], av[4 * g + 3])};
            if (one) v2 = u32x2{0x3F803F80u, 0x3F803F80u};
            st64(va + ((g ^ ph) * 16), v2);
        }
        u32x4 pos0, pos1, neg0, neg1;
        hedgehog(p, pos0, pos1, neg0, neg1);
        const int fr = (r32 & 7) ^ (((r32 >> 3) & 1) << 2);
        const unsigned rowa = L_XS0 + (w * 32 + r32) * 128;
        st128(rowa + (((0 + hh * 2 + 0) ^ fr) * 16), pos0);
        st128(rowa + (((0 + hh * 2 + 1) ^ fr) * 16), pos1);
        st128(rowa + (((4 + hh * 2 + 0) ^ fr) * 16), neg0);
        st128(rowa + (((4 + hh * 2 + 1) ^ fr) * 16), neg1);
    }
    for (unsigned spin = 0; spin < SPIN_CAP && !__all(faw == tag); ++spin) { __builtin_amdgcn_s_sleep(2); faw = __hip_atomic_load(fap, RLX_AGENT); }
    __builtin_amdgcn_fence(__ATOMIC_ACQUIRE, "wavefront");
    STAMP(4);
    {
        const int cq = w < 7 ? w : 6;
        const unsigned ga = L_GATE + (cq * 64 + (l >> 2)) * 2;
        const unsigned g0 = *(LAS const unsigned short*)ga, g1 = *(LAS const unsigned short*)(ga + 32), g2 = *(LAS const unsigned short*)(ga + 64), g3 = *(LAS const unsigned short*)(ga + 96);
        asm volatile("s_waitcnt vmcnt(0)" : "+v"(wq0), "+v"(wq1), "+v"(wq2), "+v"(wq3) :: "memory");
        if ((l & 3) == 3) {
            wq0.z = (wq0.z & 0xFFFF0000u) | g0; wq1.z = (wq1.z & 0xFFFF0000u) | g1; wq2.z = (wq2.z & 0xFFFF0000u) | g2; wq3.z = (wq3.z & 0xFFFF0000u) | g3;
        }
        const unsigned qa = L_XS2 + w * 4096 + l * 16;
        st128(qa, wq0); st128(qa + 1024, wq1); st128(qa + 2048, wq2); st128(qa + 3072, wq3);
    }
    if (!NOCLR) {
        asm volatile("buffer_store_dwordx4 %0, %1, %2, 0 offen sc1\n\ts_nop 1" :: "v"(e4), "v"(o_off + l * 16), "s"(rs_ws) : "memory");
        const u32x3 e3 = {EMPTY, EMPTY, EMPTY};
        asm volatile("buffer_store_dwordx3 %0, %1, %2, 0 offen sc1\n\ts_nop 1" :: "v"(e3), "v"(o_off + 1024 + l * 12), "s"(rs_ws) : "memory");
    }
    barrier();
    STAMP(5);
    {
        const int ft = w >> 1, dt = w & 1;
        const int i16 = l & 15, q = i16 >> 2, p4 = i16 & 3, g = l >> 4;
        const int c16 = 2 * ft + (p4 >> 1);
        const unsigned a0 = L_XS0 + (8 * g + q) * 128 + ((c16 ^ (q ^ ((g & 1) << 2))) * 16) + 8 * (p4 & 1);
        const unsigned a1 = L_XS0 + (8 * g + 4 + q) * 128 + ((c16 ^ ((4 + q) ^ ((g & 1) << 2))) * 16) + 8 * (p4 & 1);
        const int d = 16 * dt + i16;
        const int ph = (0x78 >> (2 * ((d >> 2) & 3))) & 3;
        const unsigned b0 = L_XS1 + d * 64 + ((g ^ ph) * 16);
        u32x2 lo[8], hi[8]; u32x4 bv[8];
#pragma unroll
        for (int t = 0; t < 8; ++t) { lo[t] = trd(a0 + t * 4096); hi[t] = trd(a1 + t * 4096); bv[t] = ld128(b0 + t * 4096); }
        SB();
        f32x4 kv = {}, kw = {};
#pragma unroll
        for (int t = 0; t < 8; t += 2) {
            kv = mfma16(u32x4{lo[t].x, lo[t].y, hi[t].x, hi[t].y}, bv[t], kv);
            kw = mfma16(u32x4{lo[t + 1].x, lo[t + 1].y, hi[t + 1].x, hi[t + 1].y}, bv[t + 1], kw);
        }
        kv += kw;
        const u32x4 kb = __builtin_bit_cast(u32x4, kv);
        __builtin_amdgcn_raw_buffer_store_b128(u32x4{nz(kb.x), nz(kb.y), nz(kb.z), nz(kb.w)}, rs_ws, part_off, 0, AUX_SC1);
    }
    barrier();
    STAMP(6);
    f32x16 aq = {};
    {
        const int b3 = w >> 2, G = w & 3;
        const __amdgpu_buffer_rsrc_t rs_o = __builtin_amdgcn_make_buffer_rsrc((void*)(b3 ? P.wo1 : P.wo0), 0, has3 ? DM * XROW : 0, RSRC3);
        const int j = l >> 3;
        const unsigned vch = ((l & 7) ^ (((j >> 1) & 1) << 2)) * 16;
        const unsigned so = G * 112 * XROW + ct3 * 128, m0b = L_WKV + (b3 * 28 + 7 * G) * 2048;
        u32x4 fq[3];
        fq[0] = trfrag(L_XS2 + trl); fq[1] = trfrag(L_XS2 + trl + 1024);
#pragma unroll
        for (int ks = 0; ks < 28; ++ks) {
            if (ks + 2 < 28) fq[(ks + 2) % 3] = trfrag(L_XS2 + trl + (ks + 2) * 1024);
            if ((ks & 1) == 0) {
                const int m = ks >> 2, hp = (ks >> 1) & 1;
                const int n56 = 8 * m + j, h4 = n56 / 14;
                dma16<0>(m0b + m * 2048 + hp * 1024, (n56 + 14 * (h4 + hp)) * XROW + vch, rs_o, so);
            }
            if (ks == FA2STEP) {
                wait_vm<1 + FA2STEP / 2 + 1>();
                if (l == 0) __hip_atomic_store((GAS u64*)((GAS char*)P.ws + WS_FA2) + (rb * 8 + w) * 32 + bh, tag, RLX_AGENT);
            }
            SB();
            aq = mfma32(fq[ks % 3], xf[ks], aq);
            SB();
        }
    }
    STAMP(7);
    const int pbase = WS_PART + (bh * 8 + w) * 8192 + l * 16;
    u32x4 pr[8];
#pragma unroll
    for (int r = 0; r < 8; ++r) pr[r] = __builtin_amdgcn_raw_buffer_load_b128(rs_ws, pbase + r * 1024, 0, AUX_SC1);
    const GAS u64* fbp = (const GAS u64*)((GAS char*)P.ws + WS_FA2) + (rt3 * 2 + (w & 1)) * 32 + ((w >> 1) & 1) * 16 + (l & 15);
    u64 fbw = __hip_atomic_load(fbp, RLX_AGENT);
    SB();
    u32x4 qp0, qp1, qn0, qn1;
    float gsc;
    {
        f32x16 p = {};
        p = mfma32(wp0, pack8(aq, 0), p);
        p = mfma32(wp1, pack8(aq, 1), p);
        const float zl = xupper(aq[12]);
        gsc = __builtin_amdgcn_rcpf(1.f + __builtin_amdgcn_exp2f((br ? zl : -zl) * 1.4426950408889634f));
        hedgehog(p, qp0, qp1, qn0, qn1);
    }
    SB();
    STAMPX(8);
    {
        for (unsigned spin = 0; spin < SPIN_CAP; ++spin) {
            unsigned m = 0;
#pragma unroll
            for (int r = 0; r < 8; ++r) m = umax4(m, pr[r]);
            if (!__any(m == EMPTY)) break;
            asm volatile("" ::: "memory");
#pragma unroll
            for (int r = 0; r < 8; ++r) pr[r] = __builtin_amdgcn_raw_buffer_load_b128(rs_ws, pbase + r * 1024, 0, AUX_SC1);
        }
        STAMPX(9);
        f32x4 s = (__builtin_bit_cast(f32x4, pr[0]) + __builtin_bit_cast(f32x4, pr[1])) + (__builtin_bit_cast(f32x4, pr[2]) + __builtin_bit_cast(f32x4, pr[3]));
        s += (__builtin_bit_cast(f32x4, pr[4]) + __builtin_bit_cast(f32x4, pr[5])) + (__builtin_bit_cast(f32x4, pr[6]) + __builtin_bit_cast(f32x4, pr[7]));
        const int ft = w >> 1, dt = w & 1, d = 16 * dt + (l & 15);
        st64(L_KVT + d * 128 + (((2 * ft + (l >> 5)) ^ (d & 7)) * 16) + ((l >> 4) & 1) * 8, u32x2{pk2(s[0], s[1]), pk2(s[2], s[3])});
    }
    if (has3 && w < 4) {
        for (unsigned spin = 0; spin < SPIN_CAP && !__all(fbw == tag); ++spin) { __builtin_amdgcn_s_sleep(2); fbw = __hip_atomic_load(fbp, RLX_AGENT); }
        __builtin_amdgcn_fence(__ATOMIC_ACQUIRE, "wavefront");
    }
    wait_vm<0>();
    barrier();
    STAMPX(10);
    {
        const int hp = (r32 >> 2) & 1, rg = (r32 & 3) + 4 * (r32 >> 3);
        const int d = rg < 14 ? 14 * hp + rg : 28;
        const unsigned ka = L_KVT + d * 128;
        const u32x4 a0 = ld128(ka + (((0 + hh * 2 + 0) ^ (d & 7)) * 16)), a1 = ld128(ka + (((0 + hh * 2 + 1) ^ (d & 7)) * 16));
        const u32x4 a2 = ld128(ka + (((4 + hh * 2 + 0) ^ (d & 7)) * 16)), a3 = ld128(ka + (((4 + hh * 2 + 1) ^ (d & 7)) * 16));
        f32x16 nm = {};
        nm = mfma32(a0, qp0, nm);
        nm = mfma32(a1, qp1, nm);
        nm = mfma32(a2, qn0, nm);
        nm = mfma32(a3, qn1, nm);
        const float sc = gsc * __builtin_amdgcn_rcpf(nm[14] + 1e-6f);
        const u32x4 o0 = {nz(pk2(nm[0] * sc, nm[1] * sc)), nz(pk2(nm[2] * sc, nm[3] * sc)), nz(pk2(nm[4] * sc, nm[5] * sc)), nz(pk2(nm[6] * sc, nm[7] * sc))};
        const u32x3 o1 = {nz(pk2(nm[8] * sc, nm[9] * sc)), nz(pk2(nm[10] * sc, nm[11] * sc)), nz(pk2(nm[12] * sc, nm[13] * sc))};
        __builtin_amdgcn_raw_buffer_store_b128(o0, rs_ws, o_off + l * 16, 0, AUX_SC1);
        __builtin_amdgcn_raw_buffer_store_b96(o1, rs_ws, o_off + 1024 + l * 12, 0, AUX_SC1);
    }
    STAMPX(11);
    if (has3) {
        if (w < 4) {
            const int st = w & 1, b3 = w >> 1, T = rt3 * 2 + st;
            if (DELAY2) __builtin_amdgcn_s_sleep(DELAY2);
            u32x4 oa[16]; u32x3 ob[16];
            const int obase = WS_O + (T * 32 + b3 * 16) * OBLK;
#pragma unroll
            for (int h = 0; h < 16; ++h) {
                oa[h] = __builtin_amdgcn_raw_buffer_load_b128(rs_ws, obase + h * OBLK + l * 16, 0, AUX_SC1);
                ob[h] = __builtin_amdgcn_raw_buffer_load_b96(rs_ws, obase + h * OBLK + 1024 + l * 12, 0, AUX_SC1);
            }
            SB();
            f32x16 c0 = {}, c1 = {};
            const int q4 = (l & 15) >> 2;
            const unsigned wl = L_WKV + b3 * 57344 + (8 * hh + q4) * 128 + ((l >> 4) & 1) * 32 + (l & 3) * 8;
            const unsigned w0a = wl + ((q4 >> 1) & 1) * 64, w1a = wl + (1 - ((q4 >> 1) & 1)) * 64;
#pragma unroll
            for (int G = 0; G < 4; ++G) {
                for (unsigned spin = 0; spin < SPIN_CAP; ++spin) {
                    unsigned m = 0;
#pragma unroll
                    for (int h = 4 * G; h < 4 * G + 4; ++h) m = umax3(umax3(umax4(m, oa[h]), ob[h].x, ob[h].y), ob[h].z, 0u);
                    if (!__any(m == EMPTY)) break;
                    asm volatile("" ::: "memory");
#pragma unroll
                    for (int h = 4 * G; h < 4 * G + 4; ++h) {
                        oa[h] = __builtin_amdgcn_raw_buffer_load_b128(rs_ws, obase + h * OBLK + l * 16, 0, AUX_SC1);
                        ob[h] = __builtin_amdgcn_raw_buffer_load_b96(rs_ws, obase + h * OBLK + 1024 + l * 12, 0, AUX_SC1);
                    }
                }
                if (G == 0) STAMPX(12);
                unsigned dw[28];
#pragma unroll
                for (int h = 0; h < 4; ++h) {
                    dw[7 * h] = oa[4 * G + h].x; dw[7 * h + 1] = oa[4 * G + h].y; dw[7 * h + 2] = oa[4 * G + h].z; dw[7 * h + 3] = oa[4 * G + h].w;
                    dw[7 * h + 4] = ob[4 * G + h].x; dw[7 * h + 5] = ob[4 * G + h].y; dw[7 * h + 6] = ob[4 * G + h].z;
                }
                u32x4 a0[2], a1[2];
                { const u32x2 x0 = trd(w0a + 7 * G * 2048), x1 = trd(w0a + 7 * G * 2048 + 512), y0 = trd(w1a + 7 * G * 2048), y1 = trd(w1a + 7 * G * 2048 + 512);
                  a0[0] = u32x4{x0.x, x0.y, x1.x, x1.y}; a1[0] = u32x4{y0.x, y0.y, y1.x, y1.y}; }
#pragma unroll
                for (int m = 0; m < 7; ++m) {
                    if (m < 6) {
                        const int f = 7 * G + m + 1;
                        const u32x2 x0 = trd(w0a + f * 2048), x1 = trd(w0a + f * 2048 + 512), y0 = trd(w1a + f * 2048), y1 = trd(w1a + f * 2048 + 512);
                        a0[(m + 1) & 1] = u32x4{x0.x, x0.y, x1.x, x1.y}; a1[(m + 1) & 1] = u32x4{y0.x, y0.y, y1.x, y1.y};
                    }
                    SB();
                    const u32x4 bf = {dw[4 * m], dw[4 * m + 1], dw[4 * m + 2], dw[4 * m + 3]};
                    c0 = mfma32(a0[m & 1], bf, c0);
                    c1 = mfma32(a1[m & 1], bf, c1);
                    SB();
                }
            }
            const unsigned ra = L_XS2 + (b3 * 4 + st * 2) * 4096 + r32 * 128;
#pragma unroll
            for (int g = 0; g < 4; ++g) {
                const unsigned ch = ((2 * g + hh) ^ (r32 & 7)) * 16;
                st128(ra + ch, __builtin_bit_cast(u32x4, f32x4{c0[4 * g], c0[4 * g + 1], c0[4 * g + 2], c0[4 * g + 3]}));
                st128(ra + 4096 + ch, __builtin_bit_cast(u32x4, f32x4{c1[4 * g], c1[4 * g + 1], c1[4 * g + 2], c1[4 * g + 3]}));
            }
        }
        STAMPX(13);
        barrier();
        {
            const int row = tid >> 3, c8 = tid & 7, st = row >> 5, s = row & 31, nt = c8 >> 2, ch = 2 * (c8 & 3);
            const unsigned ra = L_XS2 + (st * 2 + nt) * 4096 + s * 128;
            const unsigned o0 = (ch ^ (s & 7)) * 16, o1 = ((ch + 1) ^ (s & 7)) * 16;
            const f32x4 a0 = __builtin_bit_cast(f32x4, ld128(ra + o0)) + __builtin_bit_cast(f32x4, ld128(ra + 16384 + o0));
            const f32x4 a1 = __builtin_bit_cast(f32x4, ld128(ra + o1)) + __builtin_bit_cast(f32x4, ld128(ra + 16384 + o1));
            const u32x4 o = {pk2(a0[0], a0[1]), pk2(a0[2], a0[3]), pk2(a1[0], a1[1]), pk2(a1[2], a1[3])};
            *(GAS u32x4*)((GAS char*)P.out + (rt3 * 64 + row) * XROW + ct3 * 128 + c8 * 16) = o;
        }
        if (bid == 0 && tid == 0) __hip_atomic_store((GAS u64*)((GAS char*)P.ws + WS_E), tag, RLX_AGENT);
    }
    STAMPX(14);
}

extern "C" void kernel_launch(void* const* d_in, const int* in_sizes, int n_in, void* d_out, int out_size, void* d_ws, size_t ws_size,
                              hipStream_t stream) {
    Params P;
    P.x = d_in[0]; P.wqkv0 = d_in[1]; P.wphi0 = d_in[2]; P.wo0 = d_in[3];
    P.wqkv1 = d_in[5]; P.wphi1 = d_in[6]; P.wo1 = d_in[7]; P.gate = d_in[8];
    P.out = d_out; P.ws = d_ws;
    hipFuncSetAttribute((const void*)hedgehog_mix_kernel, hipFuncAttributeMaxDynamicSharedMemorySize, LDS_BYTES);
    hedgehog_mix_kernel<<<256, 512, LDS_BYTES, stream>>>(P);
}
```

```cpp
#include <hip/hip_runtime.h>
#include <hip/hip_bf16.h>
#include <stdint.h>

#define STAMPS 0
#define MAP1 2

typedef __attribute__((ext_vector_type(8))) __bf16 bf16x8;
typedef __attribute__((ext_vector_type(2))) __bf16 bf16x2;
typedef __attribute__((ext_vector_type(4))) short s16x4;
typedef __attribute__((ext_vector_type(16))) float f32x16;
typedef __attribute__((ext_vector_type(4))) float f32x4;
typedef __attribute__((ext_vector_type(4))) unsigned u32x4;
typedef __attribute__((ext_vector_type(3))) unsigned u32x3;
typedef __attribute__((ext_vector_type(2))) unsigned u32x2;
typedef unsigned long long u64;
#define LAS __attribute__((address_space(3)))
#define GAS __attribute__((address_space(1)))
#define DI __device__ __forceinline__
#define RLX_AGENT __ATOMIC_RELAXED, __HIP_MEMORY_SCOPE_AGENT

constexpr int SEQ = 2048, DM = 448, DH = 28;
constexpr int XROW = DM * 2;
constexpr int WROW = 3 * DM * 2;
constexpr int AUX_SC1 = 16, RSRC3 = 0x00020000;

constexpr int L_WKV = 0;
constexpr int L_XS0 = 57344;
constexpr int L_XS1 = L_XS0 + 32768;
constexpr int L_XS2 = L_XS1 + 32768;
constexpr int L_WPHI = 155648;
constexpr int L_GATE = L_WPHI + 2048;
constexpr int L_E = L_GATE + 1024;
constexpr int L_KVT = L_WPHI + 4096;
constexpr int LDS_BYTES = 163840;

constexpr int WS_E = 0;
constexpr int WS_FA1 = 4096;
constexpr int WS_FA2 = WS_FA1 + 16384;
constexpr int WS_PART = 65536;
constexpr int WS_O = WS_PART + 32 * 64 * 1024;
constexpr int OBLK = 1792;
constexpr int WS_END = WS_O + 64 * 32 * OBLK;
constexpr int WS_STAMP = 8 << 20;
constexpr unsigned SPIN_CAP = 1u << 14;
constexpr unsigned EMPTY = 0xFFFFFFFFu;
#define WPC 1
#define FA2STEP 20
#define NOX 0
#define NOW 0
#define PFC 0
#define PFD 0
#define PBAR 0
#define NOCLR 0
#define CHST 0
#define DELAY2 0

struct Params {
    const void* x; const void* wqkv0; const void* wqkv1; const void* wphi0; const void* wphi1;
    const void* wo0; const void* wo1; const void* gate; void* out; void* ws;
};

DI unsigned pk2(float a, float b) { bf16x2 v = {(__bf16)a, (__bf16)b}; return __builtin_bit_cast(unsigned, v); }
DI f32x16 mfma32(u32x4 a, u32x4 b, f32x16 c) {
    return __builtin_amdgcn_mfma_f32_32x32x16_bf16(__builtin_bit_cast(bf16x8, a), __builtin_bit_cast(bf16x8, b), c, 0, 0, 0);
}
DI f32x4 mfma16(u32x4 a, u32x4 b, f32x4 c) {
    return __builtin_amdgcn_mfma_f32_16x16x32_bf16(__builtin_bit_cast(bf16x8, a), __builtin_bit_cast(bf16x8, b), c, 0, 0, 0);
}
DI u32x2 trd(unsigned a) { return __builtin_bit_cast(u32x2, __builtin_amdgcn_ds_read_tr16_b64_v4i16((LAS s16x4*)a)); }
DI u32x4 trfrag(unsigned a) { u32x2 lo = trd(a), hi = trd(a + 256); return u32x4{lo.x, lo.y, hi.x, hi.y}; }
DI u32x4 ld128(unsigned a) { return *(LAS const u32x4*)a; }
DI void st128(unsigned a, u32x4 v) { *(LAS u32x4*)a = v; }
DI void st64(unsigned a, u32x2 v) { *(LAS u32x2*)a = v; }
template <int OFF> DI void dma16(unsigned m0, unsigned voff, __amdgpu_buffer_rsrc_t srd, unsigned soff) {
    asm volatile("s_mov_b32 m0, %0\n\ts_nop 0\n\tbuffer_load_dwordx4 %1, %2, %3 offen offset:%4 lds"
                 :: "s"(m0), "v"(voff), "s"(srd), "s"(soff), "i"(OFF) : "memory");
}
DI u32x4 gld16(unsigned voff, __amdgpu_buffer_rsrc_t srd, unsigned soff) {
    u32x4 r; asm volatile("buffer_load_dwordx4 %0, %1, %2, %3 offen" : "=v"(r) : "v"(voff), "s"(srd), "s"(soff) : "memory"); return r;
}
DI void dma4(unsigned m0, unsigned voff, __amdgpu_buffer_rsrc_t srd) {
    asm volatile("s_mov_b32 m0, %0\n\ts_nop 0\n\tbuffer_load_dword %1, %2, 0 offen sc1 lds" :: "s"(m0), "v"(voff), "s"(srd) : "memory");
}
template <int N> DI void wait_vm() { asm volatile("s_waitcnt vmcnt(%0)" :: "i"(N) : "memory"); }
DI void barrier() { asm volatile("s_waitcnt lgkmcnt(0)\n\ts_barrier" ::: "memory"); }
DI float xmax(float v) { auto r = __builtin_amdgcn_permlane32_swap(__float_as_uint(v), __float_as_uint(v), false, false); return __builtin_fmaxf(__uint_as_float(r[0]), __uint_as_float(r[1])); }
DI float xmin(float v) { auto r = __builtin_amdgcn_permlane32_swap(__float_as_uint(v), __float_as_uint(v), false, false); return __builtin_fminf(__uint_as_float(r[0]), __uint_as_float(r[1])); }
DI float xsum(float v) { auto r = __builtin_amdgcn_permlane32_swap(__float_as_uint(v), __float_as_uint(v), false, false); return __uint_as_float(r[0]) + __uint_as_float(r[1]); }
DI float xupper(float v) { auto r = __builtin_amdgcn_permlane32_swap(__float_as_uint(v), __float_as_uint(v), false, false); return __uint_as_float(r[1]); }

#define STAMP(i) do {} while (0)

#define STAMPX(i) STAMP(i)
DI void hedgehog(const f32x16& p, u32x4& pos0, u32x4& pos1, u32x4& neg0, u32x4& neg1) {
    constexpr float C = 1.4426950408889634f;
    float mx = __builtin_fmaxf(p[0], p[1]), mn = __builtin_fminf(p[0], p[1]);
#pragma unroll
    for (int i = 2; i < 14; i += 2) { mx = __builtin_fmaxf(__builtin_fmaxf(mx, p[i]), p[i + 1]); mn = __builtin_fminf(__builtin_fminf(mn, p[i]), p[i + 1]); }
    mx = xmax(mx); mn = xmin(mn);
    const float a = -mx * C, b = mn * C;
    float ep[14], en[14];
#pragma unroll
    for (int i = 0; i < 14; ++i) { ep[i] = __builtin_amdgcn_exp2f(__builtin_fmaf(p[i], C, a)); en[i] = __builtin_amdgcn_exp2f(__builtin_fmaf(p[i], -C, b)); }
    float sp = 0.f, sn = 0.f;
#pragma unroll
    for (int i = 0; i < 14; ++i) { sp += ep[i]; sn += en[i]; }
    const float rp = __builtin_amdgcn_rcpf(xsum(sp)), rn = __builtin_amdgcn_rcpf(xsum(sn));
    pos0 = u32x4{pk2(ep[0] * rp, ep[1] * rp), pk2(ep[2] * rp, ep[3] * rp), pk2(ep[4] * rp, ep[5] * rp), pk2(ep[6] * rp, ep[7] * rp)};
    pos1 = u32x4{pk2(ep[8] * rp, ep[9] * rp), pk2(ep[10] * rp, ep[11] * rp), pk2(ep[12] * rp, ep[13] * rp), 0u};
    neg0 = u32x4{pk2(en[0] * rn, en[1] * rn), pk2(en[2] * rn, en[3] * rn), pk2(en[4] * rn, en[5] * rn), pk2(en[6] * rn, en[7] * rn)};
    neg1 = u32x4{pk2(en[8] * rn, en[9] * rn), pk2(en[10] * rn, en[11] * rn), pk2(en[12] * rn, en[13] * rn), 0u};
}
DI u32x4 pack8(const f32x16& a, int s) { return u32x4{pk2(a[8 * s], a[8 * s + 1]), pk2(a[8 * s + 2], a[8 * s + 3]), pk2(a[8 * s + 4], a[8 * s + 5]), pk2(a[8 * s + 6], a[8 * s + 7])}; }

DI unsigned nz(unsigned v) { return v < EMPTY - 1 ? v : EMPTY - 1; }
DI unsigned umax3(unsigned a, unsigned b, unsigned c) { unsigned m = a > b ? a : b; return m > c ? m : c; }
DI unsigned umax4(unsigned m, u32x4 v) { return umax3(umax3(m, v.x, v.y), v.z, v.w); }
#define SB() __builtin_amdgcn_sched_barrier(0)

__global__ void __launch_bounds__(512, 2) hedgehog_mix_kernel(Params P) {
    STAMP(0);
    const int tid = threadIdx.x, l = tid & 63;
    const int w = __builtin_amdgcn_readfirstlane(tid >> 6);
    const int bid = blockIdx.x, xcd = bid & 7, idx = bid >> 3;
    const int rb = (xcd & 1) * 4 + (idx & 3), bh = (xcd >> 1) * 8 + (idx >> 2);
    const int br = bh >> 4, hd = bh & 15;
    const bool has3 = idx < 28;
    const int rt3 = xcd * 4 + (idx & 3), ct3 = idx >> 2;
    const int r32 = l & 31, hh = l >> 5;

    const __amdgpu_buffer_rsrc_t rs_x = __builtin_amdgcn_make_buffer_rsrc((void*)P.x, 0, NOX ? 0 : SEQ * XROW, RSRC3);
    const __amdgpu_buffer_rsrc_t rs_w = __builtin_amdgcn_make_buffer_rsrc((void*)(br ? P.wqkv1 : P.wqkv0), 0, NOW ? 0 : DM * WROW, RSRC3);
    const __amdgpu_buffer_rsrc_t rs_ws = __builtin_amdgcn_make_buffer_rsrc(P.ws, 0, WS_END, RSRC3);

    const unsigned vw = (l >> 2) * WROW + (l & 3) * 16;
    const unsigned xrow0 = (rb * 256 + w * 32) * XROW;
    const unsigned vxe = (l >> 3) * XROW + (((l & 7) ^ (l >> 4)) * 16), vxo = (l >> 3) * XROW + (((l & 7) ^ (4 + (l >> 4))) * 16);
    const unsigned wso = (w & 3) * 16 * WROW + (1 + (w >> 2)) * XROW + hd * 56, wm0 = L_WKV + (w >> 2) * 28672 + (w & 3) * 1024;
    auto load_w = [&](int c) { return gld16(vw, rs_w, wso + c * 64 * WROW); };
    const unsigned wst = wm0 + l * 16;
    auto issue_x = [&](int c) {
        const unsigned m0 = L_XS0 + (c % 3) * 32768 + w * 4096, so = xrow0 + c * 128;
        dma16<0>(m0, vxe, rs_x, so);
        dma16<1024>(m0, vxo, rs_x, so + 8 * XROW - 1024);
        dma16<2048>(m0, vxe, rs_x, so + 16 * XROW - 2048);
        dma16<3072>(m0, vxo, rs_x, so + 24 * XROW - 3072);
    };
    asm volatile("s_nop 4" ::: "memory");
    if (w == 0) {
        dma4(L_E, (l & 1) * 4, rs_ws);
    } else if (w == 1) {
        const __amdgpu_buffer_rsrc_t rs_g = __builtin_amdgcn_make_buffer_rsrc((void*)P.gate, 0, XROW, RSRC3);
        dma16<0>(L_GATE, l * 16, rs_g, 0);
    } else if (w < 4) {
        const __amdgpu_buffer_rsrc_t rs_p = __builtin_amdgcn_make_buffer_rsrc((void*)(br ? P.wphi1 : P.wphi0), 0, 16 * DH * DH * 2, RSRC3);
        dma16<0>(L_WPHI + (w - 2) * 1024, l * 16, rs_p, hd * (DH * DH * 2) + (w - 2) * 1024);
    }
    u32x4 wa0 = load_w(0), wa1 = load_w(1), wa2 = load_w(2);
    issue_x(0); issue_x(1);
    const int part_off = WS_PART + ((bh * 8 + w) * 8 + rb) * 1024 + l * 16;
    const int o_off = WS_O + ((rb * 8 + w) * 32 + bh) * OBLK;
    const u32x4 e4 = {EMPTY, EMPTY, EMPTY, EMPTY};
    if (!NOCLR) asm volatile("buffer_store_dwordx4 %0, %1, %2, 0 offen sc1\n\ts_nop 1" :: "v"(e4), "v"(part_off), "s"(rs_ws) : "memory");
    STAMP(1);

    const unsigned trl = (8 * hh + ((l & 15) >> 2)) * 64 + ((l >> 4) & 1) * 32 + (l & 3) * 8;
    unsigned xa[4];
#pragma unroll
    for (int t = 0; t < 4; ++t) xa[t] = L_XS0 + w * 4096 + r32 * 128 + (((hh ^ ((r32 >> 1) & 7)) * 16) ^ (32 * t));
    u32x4 xf[28];
    u32x4 wp0, wp1;
    u64 tag = 0;
    f32x16 ak = {}, av = {};
    constexpr int CL = NOCLR ? 0 : 1;
    u32x4 wb3, wb4, wb5, wb6, wq0, wq1, wq2, wq3;
    asm volatile("s_waitcnt vmcnt(%3)" : "+v"(wa0), "+v"(wa1), "+v"(wa2) : "i"(8 + CL) : "memory");
    st128(wst, wa0); st128(wst + 4096, wa1); st128(wst + 8192, wa2);
#pragma unroll
    for (int c = 0; c < 7; ++c) {
        if (c == 0) wait_vm<4 + CL>(); else if (c == 1) wait_vm<8 + CL>(); else if (c == 3) wait_vm<5>(); else wait_vm<4>();
        if (c == 2) {
            asm volatile("" : "+v"(wb3), "+v"(wb4), "+v"(wb5), "+v"(wb6));
            st128(wst + 3 * 4096, wb3); st128(wst + 4 * 4096, wb4); st128(wst + 5 * 4096, wb5); st128(wst + 6 * 4096, wb6);
        }
        if (c == 0 || c == 3) barrier();
        if (c == 0) STAMP(2);
        if (c == 2 && l == 0) __hip_atomic_store((GAS u64*)((GAS char*)P.ws + WS_FA1) + (bh * 8 + w) * 8 + rb, tag, RLX_AGENT);
#pragma unroll
        for (int t = 0; t < 4; ++t) xf[4 * c + t] = ld128(xa[t] + (c % 3) * 32768);
        u32x4 fk[2], fv[2];
        fk[0] = trfrag(L_WKV + trl + 4 * c * 1024); fv[0] = trfrag(L_WKV + 28672 + trl + 4 * c * 1024);
        SB();
        asm volatile("" : "+v"(xf[4 * c]), "+v"(xf[4 * c + 1]), "+v"(xf[4 * c + 2]), "+v"(xf[4 * c + 3]));
        if (c == 0) { wb3 = load_w(3); wb4 = load_w(4); wb5 = load_w(5); wb6 = load_w(6); }
        if (c + 2 <= 6) issue_x(c + 2);
        else if (c == 5) {
            const unsigned so = (w < 7 ? w : 6) * 64 * WROW + hd * 56;
            wq0 = gld16(vw, rs_w, so); wq1 = gld16(vw, rs_w, so + 16 * WROW); wq2 = gld16(vw, rs_w, so + 32 * WROW); wq3 = gld16(vw, rs_w, so + 48 * WROW);
        }
        if (c == 0) tag = *(LAS const u64*)L_E + 1;
        if (c == WPC) {
            const int hp = (r32 >> 2) & 1, rg = (r32 & 3) + 4 * (r32 >> 3);
            const bool fvalid = rg < 14;
            const int f = fvalid ? 14 * hp + rg : 0;
            unsigned short e[16];
#pragma unroll
            for (int s = 0; s < 2; ++s)
#pragma unroll
                for (int j = 0; j < 8; ++j) {
                    const int d = 16 * s + 8 * (j >> 2) + 4 * hh + (j & 3);
                    const bool ok = fvalid && d < DH;
                    const unsigned short v = *(LAS const unsigned short*)(L_WPHI + ((ok ? d : 0) * DH + f) * 2);
                    e[8 * s + j] = ok ? v : (unsigned short)0;
                }
            wp0 = u32x4{e[0] | (unsigned)e[1] << 16, e[2] | (unsigned)e[3] << 16, e[4] | (unsigned)e[5] << 16, e[6] | (unsigned)e[7] << 16};
            wp1 = u32x4{e[8] | (unsigned)e[9] << 16, e[10] | (unsigned)e[11] << 16, e[12] | (unsigned)e[13] << 16, e[14] | (unsigned)e[15] << 16};
        }
        SB();
#pragma unroll
        for (int t = 0; t < 4; ++t) {
            const int ks = 4 * c + t;
            if (t < 3) { fk[(t + 1) & 1] = trfrag(L_WKV + trl + (ks + 1) * 1024); fv[(t + 1) & 1] = trfrag(L_WKV + 28672 + trl + (ks + 1) * 1024); }
            SB();
            ak = mfma32(fk[t & 1], xf[ks], ak);
            av = mfma32(xf[ks], fv[t & 1], av);
            SB();
        }
    }
    STAMP(3);
    const GAS u64* fap = (const GAS u64*)((GAS char*)P.ws + WS_FA1) + (bh * 8 + w) * 8 + (l & 7);
    u64 faw = __hip_atomic_load(fap, RLX_AGENT);
    {
        f32x16 p = {};
        p = mfma32(wp0, pack8(ak, 0), p);
        p = mfma32(wp1, pack8(ak, 1), p);
        const int ph = (0x78 >> (2 * ((r32 >> 2) & 3))) & 3;
        const unsigned va = L_XS1 + w * 4096 + r32 * 64 + 8 * hh;
        const bool one = r32 == 28;
#pragma unroll
        for (int g = 0; g < 4; ++g) {
            u32x2 v2 = {pk2(av[4 * g], av[4 * g + 1]), pk2(av[4 * g + 2], av[4 * g + 3])};
            if (one) v2 = u32x2{0x3F803F80u, 0x3F803F80u};
            st64(va + ((g ^ ph) * 16), v2);
        }
        u32x4 pos0, pos1, neg0, neg1;
        hedgehog(p, pos0, pos1, neg0, neg1);
        const int fr = (r32 & 7) ^ (((r32 >> 3) & 1) << 2);
        const unsigned rowa = L_XS0 + (w * 32 + r32) * 128;
        st128(rowa + (((0 + hh * 2 + 0) ^ fr) * 16), pos0);
        st128(rowa + (((0 + hh * 2 + 1) ^ fr) * 16), pos1);
        st128(rowa + (((4 + hh * 2 + 0) ^ fr) * 16), neg0);
        st128(rowa + (((4 + hh * 2 + 1) ^ fr) * 16), neg1);
    }
#pragma nounroll
    for (unsigned spin = 0; spin < SPIN_CAP && !__all(faw == tag); ++spin) { __builtin_amdgcn_s_sleep(2); faw = __hip_atomic_load(fap, RLX_AGENT); }
    __builtin_amdgcn_fence(__ATOMIC_ACQUIRE, "wavefront");
    STAMP(4);
    {
        const int cq = w < 7 ? w : 6;
        const unsigned ga = L_GATE + (cq * 64 + (l >> 2)) * 2;
        const unsigned g0 = *(LAS const unsigned short*)ga, g1 = *(LAS const unsigned short*)(ga + 32), g2 = *(LAS const unsigned short*)(ga + 64), g3 = *(LAS const unsigned short*)(ga + 96);
        asm volatile("s_waitcnt vmcnt(0)" : "+v"(wq0), "+v"(wq1), "+v"(wq2), "+v"(wq3) :: "memory");
        if ((l & 3) == 3) {
            wq0.z = (wq0.z & 0xFFFF0000u) | g0; wq1.z = (wq1.z & 0xFFFF0000u) | g1; wq2.z = (wq2.z & 0xFFFF0000u) | g2; wq3.z = (wq3.z & 0xFFFF0000u) | g3;
        }
        const unsigned qa = L_XS2 + w * 4096 + l * 16;
        st128(qa, wq0); st128(qa + 1024, wq1); st128(qa + 2048, wq2); st128(qa + 3072, wq3);
    }
    if (!NOCLR) {
        asm volatile("buffer_store_dwordx4 %0, %1, %2, 0 offen sc1\n\ts_nop 1" :: "v"(e4), "v"(o_off + l * 16), "s"(rs_ws) : "memory");
        const u32x3 e3 = {EMPTY, EMPTY, EMPTY};
        asm volatile("buffer_store_dwordx3 %0, %1, %2, 0 offen sc1\n\ts_nop 1" :: "v"(e3), "v"(o_off + 1024 + l * 12), "s"(rs_ws) : "memory");
    }
    barrier();
    STAMP(5);
    {
        const int ft = w >> 1, dt = w & 1;
        const int i16 = l & 15, q = i16 >> 2, p4 = i16 & 3, g = l >> 4;
        const int c16 = 2 * ft + (p4 >> 1);
        const unsigned a0 = L_XS0 + (8 * g + q) * 128 + ((c16 ^ (q ^ ((g & 1) << 2))) * 16) + 8 * (p4 & 1);
        const unsigned a1 = L_XS0 + (8 * g + 4 + q) * 128 + ((c16 ^ ((4 + q) ^ ((g & 1) << 2))) * 16) + 8 * (p4 & 1);
        const int d = 16 * dt + i16;
        const int ph = (0x78 >> (2 * ((d >> 2) & 3))) & 3;
        const unsigned b0 = L_XS1 + d * 64 + ((g ^ ph) * 16);
        u32x2 lo[8], hi[8]; u32x4 bv[8];
#pragma unroll
        for (int t = 0; t < 8; ++t) { lo[t] = trd(a0 + t * 4096); hi[t] = trd(a1 + t * 4096); bv[t] = ld128(b0 + t * 4096); }
        SB();
        f32x4 kv = {}, kw = {};
#pragma unroll
        for (int t = 0; t < 8; t += 2) {
            kv = mfma16(u32x4{lo[t].x, lo[t].y, hi[t].x, hi[t].y}, bv[t], kv);
            kw = mfma16(u32x4{lo[t + 1].x, lo[t + 1].y, hi[t + 1].x, hi[t + 1].y}, bv[t + 1], kw);
        }
        kv += kw;
        const u32x4 kb = __builtin_bit_cast(u32x4, kv);
        __builtin_amdgcn_raw_buffer_store_b128(u32x4{nz(kb.x), nz(kb.y), nz(kb.z), nz(kb.w)}, rs_ws, part_off, 0, AUX_SC1);
    }
    barrier();
    STAMP(6);
    f32x16 aq = {};
    {
        const int b3 = w >> 2, G = w & 3;
        const __amdgpu_buffer_rsrc_t rs_o = __builtin_amdgcn_make_buffer_rsrc((void*)(b3 ? P.wo1 : P.wo0), 0, has3 ? DM * XROW : 0, RSRC3);
        const int j = l >> 3;
        const unsigned vch = ((l & 7) ^ (((j >> 1) & 1) << 2)) * 16;
        const unsigned so = G * 112 * XROW + ct3 * 128, m0b = L_WKV + (b3 * 28 + 7 * G) * 2048;
        u32x4 fq[3];
        fq[0] = trfrag(L_XS2 + trl); fq[1] = trfrag(L_XS2 + trl + 1024);
#pragma unroll
        for (int ks = 0; ks < 28; ++ks) {
            if (ks + 2 < 28) fq[(ks + 2) % 3] = trfrag(L_XS2 + trl + (ks + 2) * 1024);
            if ((ks & 1) == 0) {
                const int m = ks >> 2, hp = (ks >> 1) & 1;
                const int n56 = 8 * m + j, h4 = n56 / 14;
                dma16<0>(m0b + m * 2048 + hp * 1024, (n56 + 14 * (h4 + hp)) * XROW + vch, rs_o, so);
            }
            if (ks == FA2STEP) {
                wait_vm<1 + FA2STEP / 2 + 1>();
                if (l == 0) __hip_atomic_store((GAS u64*)((GAS char*)P.ws + WS_FA2) + (rb * 8 + w) * 32 + bh, tag, RLX_AGENT);
            }
            SB();
            aq = mfma32(fq[ks % 3], xf[ks], aq);
            SB();
        }
    }
    STAMP(7);
    const int pbase = WS_PART + (bh * 8 + w) * 8192 + l * 16;
    u32x4 pr[8];
#pragma unroll
    for (int r = 0; r < 8; ++r) pr[r] = __builtin_amdgcn_raw_buffer_load_b128(rs_ws, pbase + r * 1024, 0, AUX_SC1);
    const GAS u64* fbp = (const GAS u64*)((GAS char*)P.ws + WS_FA2) + (rt3 * 2 + (w & 1)) * 32 + ((w >> 1) & 1) * 16 + (l & 15);
    u64 fbw = __hip_atomic_load(fbp, RLX_AGENT);
    SB();
    u32x4 qp0, qp1, qn0, qn1;
    float gsc;
    {
        f32x16 p = {};
        p = mfma32(wp0, pack8(aq, 0), p);
        p = mfma32(wp1, pack8(aq, 1), p);
        const float zl = xupper(aq[12]);
        gsc = __builtin_amdgcn_rcpf(1.f + __builtin_amdgcn_exp2f((br ? zl : -zl) * 1.4426950408889634f));
        hedgehog(p, qp0, qp1, qn0, qn1);
    }
    SB();
    STAMPX(8);
    {
#pragma nounroll
        for (unsigned spin = 0; spin < SPIN_CAP; ++spin) {
            unsigned m = 0;
#pragma unroll
            for (int r = 0; r < 8; ++r) m = umax4(m, pr[r]);
            if (!__any(m == EMPTY)) break;
            asm volatile("" ::: "memory");
#pragma unroll
            for (int r = 0; r < 8; ++r) pr[r] = __builtin_amdgcn_raw_buffer_load_b128(rs_ws, pbase + r * 1024, 0, AUX_SC1);
        }
        STAMPX(9);
        f32x4 s = (__builtin_bit_cast(f32x4, pr[0]) + __builtin_bit_cast(f32x4, pr[1])) + (__builtin_bit_cast(f32x4, pr[2]) + __builtin_bit_cast(f32x4, pr[3]));
        s += (__builtin_bit_cast(f32x4, pr[4]) + __builtin_bit_cast(f32x4, pr[5])) + (__builtin_bit_cast(f32x4, pr[6]) + __builtin_bit_cast(f32x4, pr[7]));
        const int ft = w >> 1, dt = w & 1, d = 16 * dt + (l & 15);
        st64(L_KVT + d * 128 + (((2 * ft + (l >> 5)) ^ (d & 7)) * 16) + ((l >> 4) & 1) * 8, u32x2{pk2(s[0], s[1]), pk2(s[2], s[3])});
    }
    if (has3 && w < 4) {
#pragma nounroll
        for (unsigned spin = 0; spin < SPIN_CAP && !__all(fbw == tag); ++spin) { __builtin_amdgcn_s_sleep(2); fbw = __hip_atomic_load(fbp, RLX_AGENT); }
        __builtin_amdgcn_fence(__ATOMIC_ACQUIRE, "wavefront");
    }
    wait_vm<0>();
    barrier();
    STAMPX(10);
    {
        const int hp = (r32 >> 2) & 1, rg = (r32 & 3) + 4 * (r32 >> 3);
        const int d = rg < 14 ? 14 * hp + rg : 28;
        const unsigned ka = L_KVT + d * 128;
        const u32x4 a0 = ld128(ka + (((0 + hh * 2 + 0) ^ (d & 7)) * 16)), a1 = ld128(ka + (((0 + hh * 2 + 1) ^ (d & 7)) * 16));
        const u32x4 a2 = ld128(ka + (((4 + hh * 2 + 0) ^ (d & 7)) * 16)), a3 = ld128(ka + (((4 + hh * 2 + 1) ^ (d & 7)) * 16));
        f32x16 nm = {};
        nm = mfma32(a0, qp0, nm);
        nm = mfma32(a1, qp1, nm);
        nm = mfma32(a2, qn0, nm);
        nm = mfma32(a3, qn1, nm);
        const float sc = gsc * __builtin_amdgcn_rcpf(nm[14] + 1e-6f);
        const u32x4 o0 = {nz(pk2(nm[0] * sc, nm[1] * sc)), nz(pk2(nm[2] * sc, nm[3] * sc)), nz(pk2(nm[4] * sc, nm[5] * sc)), nz(pk2(nm[6] * sc, nm[7] * sc))};
        const u32x3 o1 = {nz(pk2(nm[8] * sc, nm[9] * sc)), nz(pk2(nm[10] * sc, nm[11] * sc)), nz(pk2(nm[12] * sc, nm[13] * sc))};
        __builtin_amdgcn_raw_buffer_store_b128(o0, rs_ws, o_off + l * 16, 0, AUX_SC1);
        __builtin_amdgcn_raw_buffer_store_b96(o1, rs_ws, o_off + 1024 + l * 12, 0, AUX_SC1);
    }
    STAMPX(11);
    if (has3) {
        if (w < 4) {
            const int st = w & 1, b3 = w >> 1, T = rt3 * 2 + st;
            if (DELAY2) __builtin_amdgcn_s_sleep(DELAY2);
            u32x4 oa[16]; u32x3 ob[16];
            const int obase = WS_O + (T * 32 + b3 * 16) * OBLK;
#pragma unroll
            for (int h = 0; h < 16; ++h) {
                oa[h] = __builtin_amdgcn_raw_buffer_load_b128(rs_ws, obase + h * OBLK + l * 16, 0, AUX_SC1);
                ob[h] = __builtin_amdgcn_raw_buffer_load_b96(rs_ws, obase + h * OBLK + 1024 + l * 12, 0, AUX_SC1);
            }
            SB();
            f32x16 c0 = {}, c1 = {};
            const int q4 = (l & 15) >> 2;
            const unsigned wl = L_WKV + b3 * 57344 + (8 * hh + q4) * 128 + ((l >> 4) & 1) * 32 + (l & 3) * 8;
            const unsigned w0a = wl + ((q4 >> 1) & 1) * 64, w1a = wl + (1 - ((q4 >> 1) & 1)) * 64;
#pragma unroll
            for (int G = 0; G < 4; ++G) {
#pragma nounroll
                for (unsigned spin = 0; spin < SPIN_CAP; ++spin) {
                    unsigned m = 0;
#pragma unroll
                    for (int h = 4 * G; h < 4 * G + 4; ++h) m = umax3(umax3(umax4(m, oa[h]), ob[h].x, ob[h].y), ob[h].z, 0u);
                    if (!__any(m == EMPTY)) break;
                    asm volatile("" ::: "memory");
#pragma unroll
                    for (int h = 4 * G; h < 4 * G + 4; ++h) {
                        oa[h] = __builtin_amdgcn_raw_buffer_load_b128(rs_ws, obase + h * OBLK + l * 16, 0, AUX_SC1);
                        ob[h] = __builtin_amdgcn_raw_buffer_load_b96(rs_ws, obase + h * OBLK + 1024 + l * 12, 0, AUX_SC1);
                    }
                }
                if (G == 0) STAMPX(12);
                unsigned dw[28];
#pragma unroll
                for (int h = 0; h < 4; ++h) {
                    dw[7 * h] = oa[4 * G + h].x; dw[7 * h + 1] = oa[4 * G + h].y; dw[7 * h + 2] = oa[4 * G + h].z; dw[7 * h + 3] = oa[4 * G + h].w;
                    dw[7 * h + 4] = ob[4 * G + h].x; dw[7 * h + 5] = ob[4 * G + h].y; dw[7 * h + 6] = ob[4 * G + h].z;
                }
                u32x4 a0[2], a1[2];
                { const u32x2 x0 = trd(w0a + 7 * G * 2048), x1 = trd(w0a + 7 * G * 2048 + 512), y0 = trd(w1a + 7 * G * 2048), y1 = trd(w1a + 7 * G * 2048 + 512);
                  a0[0] = u32x4{x0.x, x0.y, x1.x, x1.y}; a1[0] = u32x4{y0.x, y0.y, y1.x, y1.y}; }
#pragma unroll
                for (int m = 0; m < 7; ++m) {
                    if (m < 6) {
                        const int f = 7 * G + m + 1;
                        const u32x2 x0 = trd(w0a + f * 2048), x1 = trd(w0a + f * 2048 + 512), y0 = trd(w1a + f * 2048), y1 = trd(w1a + f * 2048 + 512);
                        a0[(m + 1) & 1] = u32x4{x0.x, x0.y, x1.x, x1.y}; a1[(m + 1) & 1] = u32x4{y0.x, y0.y, y1.x, y1.y};
                    }
                    SB();
                    const u32x4 bf = {dw[4 * m], dw[4 * m + 1], dw[4 * m + 2], dw[4 * m + 3]};
                    c0 = mfma32(a0[m & 1], bf, c0);
                    c1 = mfma32(a1[m & 1], bf, c1);
                    SB();
                }
            }
            const unsigned ra = L_XS2 + (b3 * 4 + st * 2) * 4096 + r32 * 128;
#pragma unroll
            for (int g = 0; g < 4; ++g) {
                const unsigned ch = ((2 * g + hh) ^ (r32 & 7)) * 16;
                st128(ra + ch, __builtin_bit_cast(u32x4, f32x4{c0[4 * g], c0[4 * g + 1], c0[4 * g + 2], c0[4 * g + 3]}));
                st128(ra + 4096 + ch, __builtin_bit_cast(u32x4, f32x4{c1[4 * g], c1[4 * g + 1], c1[4 * g + 2], c1[4 * g + 3]}));
            }
        }
        STAMPX(13);
        barrier();
        {
            const int row = tid >> 3, c8 = tid & 7, st = row >> 5, s = row & 31, nt = c8 >> 2, ch = 2 * (c8 & 3);
            const unsigned ra = L_XS2 + (st * 2 + nt) * 4096 + s * 128;
            const unsigned o0 = (ch ^ (s & 7)) * 16, o1 = ((ch + 1) ^ (s & 7)) * 16;
            const f32x4 a0 = __builtin_bit_cast(f32x4, ld128(ra + o0)) + __builtin_bit_cast(f32x4, ld128(ra + 16384 + o0));
            const f32x4 a1 = __builtin_bit_cast(f32x4, ld128(ra + o1)) + __builtin_bit_cast(f32x4, ld128(ra + 16384 + o1));
            const u32x4 o = {pk2(a0[0], a0[1]), pk2(a0[2], a0[3]), pk2(a1[0], a1[1]), pk2(a1[2], a1[3])};
            *(GAS u32x4*)((GAS char*)P.out + (rt3 * 64 + row) * XROW + ct3 * 128 + c8 * 16) = o;
        }
        if (bid == 0 && tid == 0) __hip_atomic_store((GAS u64*)((GAS char*)P.ws + WS_E), tag, RLX_AGENT);
    }
    STAMPX(14);
}

extern "C" void kernel_launch(void* const* d_in, const int* in_sizes, int n_in, void* d_out, int out_size, void* d_ws, size_t ws_size,
                              hipStream_t stream) {
    Params P;
    P.x = d_in[0]; P.wqkv0 = d_in[1]; P.wphi0 = d_in[2]; P.wo0 = d_in[3];
    P.wqkv1 = d_in[5]; P.wphi1 = d_in[6]; P.wo1 = d_in[7]; P.gate = d_in[8];
    P.out = d_out; P.ws = d_ws;
    hipFuncSetAttribute((const void*)hedgehog_mix_kernel, hipFuncAttributeMaxDynamicSharedMemorySize, LDS_BYTES);
    hedgehog_mix_kernel<<<256, 512, LDS_BYTES, stream>>>(P);
}
```

```cpp
#include <hip/hip_runtime.h>
#include <hip/hip_bf16.h>
#include <stdint.h>

#define STAMPS 0
#define KCH 0
#define MAP1 2

typedef __attribute__((ext_vector_type(8))) __bf16 bf16x8;
typedef __attribute__((ext_vector_type(2))) __bf16 bf16x2;
typedef __attribute__((ext_vector_type(4))) short s16x4;
typedef __attribute__((ext_vector_type(16))) float f32x16;
typedef __attribute__((ext_vector_type(4))) float f32x4;
typedef __attribute__((ext_vector_type(4))) unsigned u32x4;
typedef __attribute__((ext_vector_type(3))) unsigned u32x3;
typedef __attribute__((ext_vector_type(2))) unsigned u32x2;
typedef unsigned long long u64;
#define LAS __attribute__((address_space(3)))
#define GAS __attribute__((address_space(1)))
#define DI __device__ __forceinline__
#define RLX_AGENT __ATOMIC_RELAXED, __HIP_MEMORY_SCOPE_AGENT

constexpr int SEQ = 2048, DM = 448, DH = 28;
constexpr int XROW = DM * 2;
constexpr int WROW = 3 * DM * 2;
constexpr int AUX_SC1 = 16, RSRC3 = 0x00020000;

constexpr int L_WKV = 0;
constexpr int L_XS0 = 57344;
constexpr int L_XS1 = L_XS0 + 32768;
constexpr int L_XS2 = L_XS1 + 32768;
constexpr int L_WPHI = 155648;
constexpr int L_GATE = L_WPHI + 2048;
constexpr int L_E = L_GATE + 1024;
constexpr int L_CNT = L_E + 256;
constexpr int L_KVT = L_WPHI + 4096;
constexpr int LDS_BYTES = 163840;

constexpr int WS_E = 0;
constexpr int WS_FA1 = 4096;
constexpr int WS_FA2 = WS_FA1 + 16384;
constexpr int WS_PART = 65536;
constexpr int WS_O = WS_PART + 32 * 64 * 1024;
constexpr int OBLK = 1792;
constexpr int WS_END = WS_O + 64 * 32 * OBLK;
constexpr int WS_STAMP = 8 << 20;
constexpr unsigned SPIN_CAP = 1u << 14;
constexpr unsigned EMPTY = 0xFFFFFFFFu;
#define WPC 1
#define FA2STEP 12
#define NOX 0
#define NOW 0
#define PFC 0
#define PFD 0
#define RSLEEP 4
#define PRIO 1
#define NPRE 2
#define WARM 0
#define QPF 4
#define PRT 2
#define PBAR 0
#define NOCLR 0
#define CHST 0
#define DELAY2 12

struct Params {
    const void* x; const void* wqkv0; const void* wqkv1; const void* wphi0; const void* wphi1;
    const void* wo0; const void* wo1; const void* gate; void* out; void* ws;
};

DI unsigned pk2(float a, float b) { bf16x2 v = {(__bf16)a, (__bf16)b}; return __builtin_bit_cast(unsigned, v); }
DI f32x16 mfma32(u32x4 a, u32x4 b, f32x16 c) {
    return __builtin_amdgcn_mfma_f32_32x32x16_bf16(__builtin_bit_cast(bf16x8, a), __builtin_bit_cast(bf16x8, b), c, 0, 0, 0);
}
DI f32x4 mfma16(u32x4 a, u32x4 b, f32x4 c) {
    return __builtin_amdgcn_mfma_f32_16x16x32_bf16(__builtin_bit_cast(bf16x8, a), __builtin_bit_cast(bf16x8, b), c, 0, 0, 0);
}
DI u32x2 trd(unsigned a) { return __builtin_bit_cast(u32x2, __builtin_amdgcn_ds_read_tr16_b64_v4i16((LAS s16x4*)a)); }
DI u32x4 trfrag(unsigned a) { u32x2 lo = trd(a), hi = trd(a + 256); return u32x4{lo.x, lo.y, hi.x, hi.y}; }
DI u32x4 ld128(unsigned a) { return *(LAS const u32x4*)a; }
DI void st128(unsigned a, u32x4 v) { *(LAS u32x4*)a = v; }
DI void st64(unsigned a, u32x2 v) { *(LAS u32x2*)a = v; }
template <int OFF> DI void dma16(unsigned m0, unsigned voff, __amdgpu_buffer_rsrc_t srd, unsigned soff) {
    asm volatile("s_mov_b32 m0, %0\n\ts_nop 0\n\tbuffer_load_dwordx4 %1, %2, %3 offen offset:%4 lds"
                 :: "s"(m0), "v"(voff), "s"(srd), "s"(soff), "i"(OFF) : "memory");
}
DI u32x4 gld16(unsigned voff, __amdgpu_buffer_rsrc_t srd, unsigned soff) {
    u32x4 r; asm volatile("buffer_load_dwordx4 %0, %1, %2, %3 offen" : "=v"(r) : "v"(voff), "s"(srd), "s"(soff) : "memory"); return r;
}
DI u32x4 gld16c(unsigned voff, __amdgpu_buffer_rsrc_t srd) {
    u32x4 r; asm volatile("buffer_load_dwordx4 %0, %1, %2, 0 offen sc1" : "=v"(r) : "v"(voff), "s"(srd) : "memory"); return r;
}
DI u32x2 gld8c(unsigned voff, __amdgpu_buffer_rsrc_t srd) {
    u32x2 r; asm volatile("buffer_load_dwordx2 %0, %1, %2, 0 offen sc1" : "=v"(r) : "v"(voff), "s"(srd) : "memory"); return r;
}
DI void dma4(unsigned m0, unsigned voff, __amdgpu_buffer_rsrc_t srd) {
    asm volatile("s_mov_b32 m0, %0\n\ts_nop 0\n\tbuffer_load_dword %1, %2, 0 offen sc1 lds" :: "s"(m0), "v"(voff), "s"(srd) : "memory");
}
template <int N> DI void wait_vm() { asm volatile("s_waitcnt vmcnt(%0)" :: "i"(N) : "memory"); }
DI void barrier() { asm volatile("s_waitcnt lgkmcnt(0)\n\ts_barrier" ::: "memory"); }
DI float xmax(float v) { auto r = __builtin_amdgcn_permlane32_swap(__float_as_uint(v), __float_as_uint(v), false, false); return __builtin_fmaxf(__uint_as_float(r[0]), __uint_as_float(r[1])); }
DI float xmin(float v) { auto r = __builtin_amdgcn_permlane32_swap(__float_as_uint(v), __float_as_uint(v), false, false); return __builtin_fminf(__uint_as_float(r[0]), __uint_as_float(r[1])); }
DI float xsum(float v) { auto r = __builtin_amdgcn_permlane32_swap(__float_as_uint(v), __float_as_uint(v), false, false); return __uint_as_float(r[0]) + __uint_as_float(r[1]); }
DI float xupper(float v) { auto r = __builtin_amdgcn_permlane32_swap(__float_as_uint(v), __float_as_uint(v), false, false); return __uint_as_float(r[1]); }

#define STAMP(i) do {} while (0)

#define QST 0
#define STAMPQ(i) do {} while (0)
#define P3ST 0
#define STAMPY(i) do {} while (0)
#define STAMPZ(i) STAMP(i)
#define STAMPX(i) STAMP(i)
DI float max3f(float a, float b, float c) { float r; asm("v_max3_f32 %0, %1, %2, %3" : "=v"(r) : "v"(a), "v"(b), "v"(c)); return r; }
DI float min3f(float a, float b, float c) { float r; asm("v_min3_f32 %0, %1, %2, %3" : "=v"(r) : "v"(a), "v"(b), "v"(c)); return r; }
template <class F> DI void hedgehog(const f32x16& p, u32x4& pos0, u32x4& pos1, u32x4& neg0, u32x4& neg1, F tick) {
    constexpr float C = 1.4426950408889634f;
    float mx = max3f(p[0], p[1], p[1]), mn = min3f(p[0], p[1], p[1]);
#pragma unroll
    for (int i = 2; i < 14; i += 2) { mx = max3f(mx, p[i], p[i + 1]); mn = min3f(mn, p[i], p[i + 1]); }
    { auto r = __builtin_amdgcn_permlane32_swap(__float_as_uint(mx), __float_as_uint(mx), false, false); mx = max3f(__uint_as_float(r[0]), __uint_as_float(r[1]), __uint_as_float(r[1])); }
    { auto r = __builtin_amdgcn_permlane32_swap(__float_as_uint(mn), __float_as_uint(mn), false, false); mn = min3f(__uint_as_float(r[0]), __uint_as_float(r[1]), __uint_as_float(r[1])); }
    const float a = -mx * C, b = mn * C;
    tick(0);
    float ep[14], en[14];
    float sp = 0.f, sn = 0.f;
#pragma unroll
    for (int i = 0; i < 14; ++i) {
        ep[i] = __builtin_amdgcn_exp2f(__builtin_fmaf(p[i], C, a)); en[i] = __builtin_amdgcn_exp2f(__builtin_fmaf(p[i], -C, b));
        if (i == 0) { sp = ep[0]; sn = en[0]; } else { sp += ep[i]; sn += en[i]; }
        if (i % 3 == 2) tick(1 + i / 3);
    }
    const float rp = __builtin_amdgcn_rcpf(xsum(sp)), rn = __builtin_amdgcn_rcpf(xsum(sn));
    tick(5);
    pos0 = u32x4{pk2(ep[0] * rp, ep[1] * rp), pk2(ep[2] * rp, ep[3] * rp), pk2(ep[4] * rp, ep[5] * rp), pk2(ep[6] * rp, ep[7] * rp)};
    pos1 = u32x4{pk2(ep[8] * rp, ep[9] * rp), pk2(ep[10] * rp, ep[11] * rp), pk2(ep[12] * rp, ep[13] * rp), 0u};
    tick(6);
    neg0 = u32x4{pk2(en[0] * rn, en[1] * rn), pk2(en[2] * rn, en[3] * rn), pk2(en[4] * rn, en[5] * rn), pk2(en[6] * rn, en[7] * rn)};
    neg1 = u32x4{pk2(en[8] * rn, en[9] * rn), pk2(en[10] * rn, en[11] * rn), pk2(en[12] * rn, en[13] * rn), 0u};
}
DI u32x4 pack8(const f32x16& a, int s) { return u32x4{pk2(a[8 * s], a[8 * s + 1]), pk2(a[8 * s + 2], a[8 * s + 3]), pk2(a[8 * s + 4], a[8 * s + 5]), pk2(a[8 * s + 6], a[8 * s + 7])}; }

DI unsigned nz(unsigned v) { return v < EMPTY - 1 ? v : EMPTY - 1; }
DI unsigned umax3(unsigned a, unsigned b, unsigned c) { unsigned m = a > b ? a : b; return m > c ? m : c; }
DI unsigned umax4(unsigned m, u32x4 v) { return umax3(umax3(m, v.x, v.y), v.z, v.w); }
#define SB() __builtin_amdgcn_sched_barrier(0)

__global__ void __launch_bounds__(512, 2) hedgehog_mix_kernel(Params P) {
    STAMP(0);
    const int tid = threadIdx.x, l = tid & 63;
    const int w = __builtin_amdgcn_readfirstlane(tid >> 6);
    const int bid = blockIdx.x, xcd = bid & 7, idx = bid >> 3;
    const int rb = (xcd & 1) * 4 + (idx & 3), bh = (xcd >> 1) * 8 + (idx >> 2);
    const int br = bh >> 4, hd = bh & 15;
    const bool has3 = idx < 28;
    const int rt3 = xcd * 4 + (idx & 3), ct3 = idx >> 2;
    const int r32 = l & 31, hh = l >> 5;

    const __amdgpu_buffer_rsrc_t rs_x = __builtin_amdgcn_make_buffer_rsrc((void*)P.x, 0, NOX ? 0 : SEQ * XROW, RSRC3);
    const __amdgpu_buffer_rsrc_t rs_w = __builtin_amdgcn_make_buffer_rsrc((void*)(br ? P.wqkv1 : P.wqkv0), 0, NOW ? 0 : DM * WROW, RSRC3);
    const __amdgpu_buffer_rsrc_t rs_ws = __builtin_amdgcn_make_buffer_rsrc(P.ws, 0, WS_END, RSRC3);

    const unsigned vw = (l >> 2) * WROW + (l & 3) * 16;
    const unsigned xrow0 = (rb * 256 + w * 32) * XROW;
    const unsigned vxe = (l >> 3) * XROW + (((l & 7) ^ (l >> 4)) * 16), vxo = (l >> 3) * XROW + (((l & 7) ^ (4 + (l >> 4))) * 16);
    const unsigned wso = (w & 3) * 16 * WROW + (1 + (w >> 2)) * XROW + hd * 56, wm0 = L_WKV + (w >> 2) * 28672 + (w & 3) * 1024;
    auto load_w = [&](int c) __attribute__((always_inline)) { return gld16(vw, rs_w, wso + c * 64 * WROW); };
    const unsigned wst = wm0 + l * 16;
    auto issue_x = [&](int c, int p) __attribute__((always_inline)) {
        const unsigned m0 = L_XS0 + (c % 3) * 32768 + w * 4096, so = xrow0 + c * 128;
        if (p == 0) dma16<0>(m0, vxe, rs_x, so);
        if (p == 1) dma16<1024>(m0, vxo, rs_x, so + 8 * XROW - 1024);
        if (p == 2) dma16<2048>(m0, vxe, rs_x, so + 16 * XROW - 2048);
        if (p == 3) dma16<3072>(m0, vxo, rs_x, so + 24 * XROW - 3072);
    };
    asm volatile("s_nop 4" ::: "memory");
    u32x4 wr[3];
    wr[0] = load_w(0);
    issue_x(0, 0); issue_x(0, 1); issue_x(0, 2); issue_x(0, 3);
    if (w == 0) {
        dma4(L_E, (l & 1) * 4, rs_ws);
    } else if (w == 1) {
        const __amdgpu_buffer_rsrc_t rs_g = __builtin_amdgcn_make_buffer_rsrc((void*)P.gate, 0, XROW, RSRC3);
        dma16<0>(L_GATE, l * 16, rs_g, 0);
    } else if (w < 4) {
        const __amdgpu_buffer_rsrc_t rs_p = __builtin_amdgcn_make_buffer_rsrc((void*)(br ? P.wphi1 : P.wphi0), 0, 16 * DH * DH * 2, RSRC3);
        dma16<0>(L_WPHI + (w - 2) * 1024, l * 16, rs_p, hd * (DH * DH * 2) + (w - 2) * 1024);
    } else {
        dma4(L_KVT + (w - 4) * 256, (l & 1) * 4, rs_ws);
    }
    __builtin_amdgcn_s_barrier();
    wr[1] = load_w(1);
    issue_x(1, 0); issue_x(1, 1); issue_x(1, 2); issue_x(1, 3);
    const int part_off = WS_PART + ((bh * 8 + w) * 8 + rb) * 1024 + l * 16;
    const int o_off = WS_O + ((rb * 8 + w) * 32 + bh) * OBLK;
    const u32x4 e4 = {EMPTY, EMPTY, EMPTY, EMPTY};
    asm volatile("buffer_store_dwordx4 %0, %1, %2, 0 offen sc1\n\ts_nop 1" :: "v"(e4), "v"(part_off), "s"(rs_ws) : "memory");
    if (!QST) STAMPZ(1);

    const unsigned trl = (8 * hh + ((l & 15) >> 2)) * 64 + ((l >> 4) & 1) * 32 + (l & 3) * 8;
    unsigned xa[4];
#pragma unroll
    for (int t = 0; t < 4; ++t) xa[t] = L_XS0 + w * 4096 + r32 * 128 + (((hh ^ ((r32 >> 1) & 7)) * 16) ^ (32 * t));
    u32x4 xf[28];
    u64 tag = 0;
    f32x16 ak = {}, av = {};
    unsigned a_phi = L_XS0 + (w * 32 + r32) * 128 + (((2 * hh) ^ ((r32 & 7) ^ (((r32 >> 3) & 1) << 2))) << 4);
    unsigned a_v = L_XS1 + w * 4096 + r32 * 64 + 8 * hh + (((0x78 >> (2 * ((r32 >> 2) & 3))) & 3) << 4);
    unsigned a_kva, a_kvb, a_kvv, a_kvt, a_num;
    {
        const int ft = w >> 1, dt = w & 1, i16 = l & 15, q = i16 >> 2, p4 = i16 & 3, g = l >> 4, c16 = 2 * ft + (p4 >> 1), d = 16 * dt + i16;
        a_kva = L_XS0 + (8 * g + q) * 128 + ((c16 ^ (q ^ ((g & 1) << 2))) * 16) + 8 * (p4 & 1);
        a_kvb = L_XS0 + (8 * g + 4 + q) * 128 + ((c16 ^ ((4 + q) ^ ((g & 1) << 2))) * 16) + 8 * (p4 & 1);
        a_kvv = L_XS1 + d * 64 + ((g ^ ((0x78 >> (2 * ((d >> 2) & 3))) & 3)) * 16);
        a_kvt = L_KVT + d * 128 + (((2 * ft + (l >> 5)) ^ (d & 7)) * 16) + ((l >> 4) & 1) * 8;
        const int rg = (r32 & 3) + 4 * (r32 >> 3), dn = rg < 14 ? 14 * ((r32 >> 2) & 1) + rg : 28;
        a_num = L_KVT + dn * 128 + (((2 * hh) ^ (dn & 7)) << 4);
    }
    asm volatile("" : "+v"(a_phi), "+v"(a_v), "+v"(a_kva), "+v"(a_kvb), "+v"(a_kvv), "+v"(a_kvt), "+v"(a_num));
    const GAS u64* fap = (const GAS u64*)((GAS char*)P.ws + WS_FA1) + (bh * 8 + w) * 8 + (l & 7);
    u64 faw = 0;
    asm volatile("s_waitcnt vmcnt(%1)" : "+v"(wr[0]) : "i"(6 + (WARM & 1)) : "memory");
    st128(wst, wr[0]);
    if (tid == 0) *(LAS unsigned*)L_CNT = 0;
    barrier();
    if (!QST) STAMPZ(2);
#pragma unroll
    for (int c = 0; c < 7; ++c) {
        if (c <= 4) issue_x(c + 2, 0);
        if (c == 5) faw = __hip_atomic_load(fap, RLX_AGENT);
        if (c == 6) dma16<0>(L_XS2 + w * 4096, vw, rs_w, (w < 7 ? w : 6) * 64 * WROW + hd * 56);
#pragma unroll
        for (int t = 0; t < 4; ++t) xf[4 * c + t] = ld128(xa[t] + (c % 3) * 32768);
        u32x4 fk[2], fv[2];
        fk[0] = trfrag(L_WKV + trl + 4 * c * 1024); fv[0] = trfrag(L_WKV + 28672 + trl + 4 * c * 1024);
        if (c == 0) tag = *(LAS const u64*)L_E + 1;
        SB();
#pragma unroll
        for (int t = 0; t < 4; ++t) {
            const int ks = 4 * c + t;
            if (t < 3) { fk[(t + 1) & 1] = trfrag(L_WKV + trl + (ks + 1) * 1024); fv[(t + 1) & 1] = trfrag(L_WKV + 28672 + trl + (ks + 1) * 1024); }
            SB();
            ak = mfma32(fk[t & 1], xf[ks], ak);
            av = mfma32(xf[ks], fv[t & 1], av);
            SB();
            if (c <= 4) { if (t < 3) issue_x(c + 2, t + 1); else wr[(c + 2) % 3] = load_w(c + 2); }
            if (c == 6 && t < 3) {
                const unsigned so = (w < 7 ? w : 6) * 64 * WROW + hd * 56 + (t + 1) * (16 * WROW - 1024);
                if (t == 0) dma16<1024>(L_XS2 + w * 4096, vw, rs_w, so); else if (t == 1) dma16<2048>(L_XS2 + w * 4096, vw, rs_w, so); else dma16<3072>(L_XS2 + w * 4096, vw, rs_w, so);
            }
            SB();
        }
        if (c == 0) {
            const int ln = (tid >> 2) & 63, i = ln & 31, j = 2 * (tid & 3);
            const int rg = (i & 3) + 4 * (i >> 3), f = 14 * ((i >> 2) & 1) + rg;
            const int d = 16 * (tid >> 8) + 8 * (j >> 2) + 4 * (ln >> 5) + (j & 3);
            const bool ok = rg < 14 && d < DH;
            const unsigned a = L_WPHI + (ok ? d * DH + f : 0) * 2;
            const unsigned lo = *(LAS const unsigned short*)a, hi = *(LAS const unsigned short*)(a + 2 * DH);
            *(LAS unsigned*)(L_KVT + tid * 4) = ok ? lo | hi << 16 : 0u;
            SB();
        }
        if (c < 6) {
            if (c == 1 || (c == 0 && (WARM & 1))) asm volatile("s_waitcnt vmcnt(6)" : "+v"(wr[(c + 1) % 3]) :: "memory");
            else if (c == 5) asm volatile("s_waitcnt vmcnt(0)" : "+v"(wr[(c + 1) % 3]) :: "memory");
            else asm volatile("s_waitcnt vmcnt(5)" : "+v"(wr[(c + 1) % 3]) :: "memory");
            st128(wst + (c + 1) * 4096, wr[(c + 1) % 3]);
            if (c == 0 && l == 0) __hip_atomic_store((GAS u64*)((GAS char*)P.ws + WS_FA1) + (bh * 8 + w) * 8 + rb, tag, RLX_AGENT);
            if (c == 5) {
#pragma nounroll
                for (unsigned spin = 0; spin < SPIN_CAP && !__all(faw == tag); ++spin) { __builtin_amdgcn_s_sleep(2); faw = __hip_atomic_load(fap, RLX_AGENT); }
                __builtin_amdgcn_fence(__ATOMIC_ACQUIRE, "wavefront");
            }
            barrier();
        }
    }
    STAMPZ(3);
    const u32x4 wp0 = ld128(L_KVT + l * 16), wp1 = ld128(L_KVT + 1024 + l * 16);
    {
        f32x16 p = {};
        p = mfma32(wp0, pack8(ak, 0), p);
        p = mfma32(wp1, pack8(ak, 1), p);
        const bool one = r32 == 28;
#pragma unroll
        for (int g = 0; g < 4; ++g) {
            u32x2 v2 = {pk2(av[4 * g], av[4 * g + 1]), pk2(av[4 * g + 2], av[4 * g + 3])};
            if (one) v2 = u32x2{0x3F803F80u, 0x3F803F80u};
            st64(a_v ^ (g * 16), v2);
        }
        u32x4 pos0, pos1, neg0, neg1;
        hedgehog(p, pos0, pos1, neg0, neg1, [](int) __attribute__((always_inline)) {});
        st128(a_phi, pos0); st128(a_phi ^ 16, pos1); st128(a_phi ^ 64, neg0); st128(a_phi ^ 80, neg1);
    }
    STAMPZ(4);
    {
        asm volatile("buffer_store_dwordx4 %0, %1, %2, 0 offen sc1\n\ts_nop 1" :: "v"(e4), "v"(o_off + l * 16), "s"(rs_ws) : "memory");
        const u32x3 e3 = {EMPTY, EMPTY, EMPTY};
        asm volatile("buffer_store_dwordx3 %0, %1, %2, 0 offen sc1\n\ts_nop 1" :: "v"(e3), "v"(o_off + 1024 + l * 12), "s"(rs_ws) : "memory");
    }
    barrier();
    STAMPZ(5);
    {
        u32x2 lo[8], hi[8]; u32x4 bv[8];
#pragma unroll
        for (int t = 0; t < 8; ++t) { lo[t] = trd(a_kva + t * 4096); hi[t] = trd(a_kvb + t * 4096); bv[t] = ld128(a_kvv + t * 4096); }
        const unsigned short gv = *(LAS const unsigned short*)(L_GATE + ((w < 7 ? w : 6) * 64 + l) * 2);
        SB();
        f32x4 kv = {}, kw = {};
#pragma unroll
        for (int t = 0; t < 8; t += 2) {
            kv = mfma16(u32x4{lo[t].x, lo[t].y, hi[t].x, hi[t].y}, bv[t], kv);
            kw = mfma16(u32x4{lo[t + 1].x, lo[t + 1].y, hi[t + 1].x, hi[t + 1].y}, bv[t + 1], kw);
        }
        kv += kw;
        const u32x4 kb = __builtin_bit_cast(u32x4, kv);
        __builtin_amdgcn_raw_buffer_store_b128(u32x4{nz(kb.x), nz(kb.y), nz(kb.z), nz(kb.w)}, rs_ws, part_off, 0, AUX_SC1);
        wait_vm<3>();
        *(LAS unsigned short*)(L_XS2 + w * 4096 + l * 64 + 56) = gv;
    }
    barrier();
    STAMPZ(6);
    f32x16 aq = {};
    const int jr = l >> 3;
    const unsigned vch = ((l & 7) ^ (((jr >> 1) & 1) << 2)) * 16;
    auto issue_wo = [&](__amdgpu_buffer_rsrc_t rs, unsigned m0, unsigned so, int i) __attribute__((always_inline)) {
        const int m = i >> 1, hp = i & 1, n56 = 8 * m + (m == 5 ? (jr + 2) & 7 : jr), h4 = n56 / 14;
        dma16<0>(m0 + m * 2048 + hp * 1024, (n56 + 14 * (h4 + hp)) * XROW + vch, rs, so);
    };
    {
        const __amdgpu_buffer_rsrc_t rs_o = __builtin_amdgcn_make_buffer_rsrc((void*)(w >= 4 ? P.wo1 : P.wo0), 0, has3 ? DM * XROW : 0, RSRC3);
        const unsigned wo_m0 = L_WKV + ((w >> 2) * 28 + 7 * ((w >> 1) & 1)) * 2048, wo_so = ((w >> 1) & 1) * 112 * XROW + ct3 * 128;
        u32x4 fq[QPF + 1];
#pragma unroll
        for (int i = 0; i < QPF; ++i) fq[i] = trfrag(L_XS2 + trl + i * 1024);
#pragma unroll
        for (int ks = 0; ks < 28; ++ks) {
            if (ks + QPF < 28) fq[(ks + QPF) % (QPF + 1)] = trfrag(L_XS2 + trl + (ks + QPF) * 1024);
            if ((ks & 3) == 0) { if (w & 1) issue_wo(rs_o, wo_m0, wo_so, 7 + (ks >> 2)); else issue_wo(rs_o, wo_m0, wo_so, ks >> 2); }
            if (ks == FA2STEP) {
                wait_vm<1 + FA2STEP / 4 + 1>();
                if (l == 0) __hip_atomic_store((GAS u64*)((GAS char*)P.ws + WS_FA2) + (rb * 8 + w) * 32 + bh, tag, RLX_AGENT);
            }
            SB();
            aq = mfma32(fq[ks % (QPF + 1)], xf[ks], aq);
            SB();
        }
    }
    STAMPZ(7);
    const int pbase = WS_PART + (bh * 8 + w) * 8192 + l * 16;
    u32x4 pr[8];
    u32x2 fbw;
    const unsigned fbo = WS_FA2 + ((rt3 * 2 + (w & 1)) * 32 + ((w >> 1) & 1) * 16 + (l & 15)) * 8;
    u32x4 qp0, qp1, qn0, qn1;
    float gsc;
    {
        f32x16 p = {};
        p = mfma32(wp0, pack8(aq, 0), p);
        p = mfma32(wp1, pack8(aq, 1), p);
        const float zl = xupper(aq[12]);
        gsc = __builtin_amdgcn_rcpf(1.f + __builtin_amdgcn_exp2f((br ? zl : -zl) * 1.4426950408889634f));
        hedgehog(p, qp0, qp1, qn0, qn1, [&](int i) __attribute__((always_inline)) {
            SB();
            if (i == PRT) {
#pragma unroll
                for (int r = 0; r < 8; ++r) pr[r] = gld16c(pbase + r * 1024, rs_ws);
                fbw = gld8c(fbo, rs_ws);
            }
            SB();
        });
    }
    SB();
    STAMPX(8);
    {
        asm volatile("s_waitcnt vmcnt(%9)" : "+v"(pr[0]), "+v"(pr[1]), "+v"(pr[2]), "+v"(pr[3]), "+v"(pr[4]), "+v"(pr[5]), "+v"(pr[6]), "+v"(pr[7]), "+v"(fbw) : "i"(0) : "memory");
        unsigned m = 0;
#pragma unroll
        for (int r = 0; r < 8; ++r) m = umax4(m, pr[r]);
        if (__builtin_expect(__any(m == EMPTY), 0)) {
#pragma nounroll
            for (unsigned spin = 0; spin < SPIN_CAP; ++spin) {
                __builtin_amdgcn_s_sleep(RSLEEP);
                asm volatile("" ::: "memory");
#pragma unroll
                for (int r = 0; r < 8; ++r) pr[r] = __builtin_amdgcn_raw_buffer_load_b128(rs_ws, pbase + r * 1024, 0, AUX_SC1);
                m = 0;
#pragma unroll
                for (int r = 0; r < 8; ++r) m = umax4(m, pr[r]);
                if (!__any(m == EMPTY)) break;
            }
        }
        STAMPX(9);
        f32x4 s = (__builtin_bit_cast(f32x4, pr[0]) + __builtin_bit_cast(f32x4, pr[1])) + (__builtin_bit_cast(f32x4, pr[2]) + __builtin_bit_cast(f32x4, pr[3]));
        s += (__builtin_bit_cast(f32x4, pr[4]) + __builtin_bit_cast(f32x4, pr[5])) + (__builtin_bit_cast(f32x4, pr[6]) + __builtin_bit_cast(f32x4, pr[7]));
        st64(a_kvt, u32x2{pk2(s[0], s[1]), pk2(s[2], s[3])});
    }
    barrier();
    STAMPX(10);
    {
        const u32x4 a0 = ld128(a_num), a1 = ld128(a_num ^ 16), a2 = ld128(a_num ^ 64), a3 = ld128(a_num ^ 80);
        f32x16 nm = {};
        nm = mfma32(a0, qp0, nm);
        nm = mfma32(a1, qp1, nm);
        nm = mfma32(a2, qn0, nm);
        nm = mfma32(a3, qn1, nm);
        const float sc = gsc * __builtin_amdgcn_rcpf(nm[14] + 1e-6f);
        unsigned o[7];
#pragma unroll
        for (int i = 0; i < 7; ++i) o[i] = nz(pk2(nm[2 * i] * sc, nm[2 * i + 1] * sc));
        const int hq = hd & 3;
        if (hq == 0) {
            __builtin_amdgcn_raw_buffer_store_b128(u32x4{o[0], o[1], o[2], o[3]}, rs_ws, o_off + l * 16, 0, AUX_SC1);
            __builtin_amdgcn_raw_buffer_store_b96(u32x3{o[4], o[5], o[6]}, rs_ws, o_off + 1024 + l * 12, 0, AUX_SC1);
        } else if (hq == 3) {
            __builtin_amdgcn_raw_buffer_store_b128(u32x4{o[3], o[4], o[5], o[6]}, rs_ws, o_off + l * 16, 0, AUX_SC1);
            __builtin_amdgcn_raw_buffer_store_b96(u32x3{o[0], o[1], o[2]}, rs_ws, o_off + 1024 + l * 12, 0, AUX_SC1);
        } else {
            const bool h1 = hq == 1;
            __builtin_amdgcn_raw_buffer_store_b128(h1 ? u32x4{o[1], o[2], o[3], o[4]} : u32x4{o[2], o[3], o[4], o[5]}, rs_ws, o_off + l * 16, 0, AUX_SC1);
            __builtin_amdgcn_raw_buffer_store_b64(h1 ? u32x2{o[5], o[6]} : u32x2{o[0], o[1]}, rs_ws, o_off + 1024 + l * 8, 0, AUX_SC1);
            __builtin_amdgcn_raw_buffer_store_b32(h1 ? o[0] : o[6], rs_ws, o_off + 1536 + l * 4, 0, AUX_SC1);
        }
    }
    STAMPX(11);
    if (has3) {
        if (w >= 4) {
            const int b3 = (w >> 1) & 1, G = 2 + (w & 1);
            const __amdgpu_buffer_rsrc_t rs_o = __builtin_amdgcn_make_buffer_rsrc((void*)(b3 ? P.wo1 : P.wo0), 0, DM * XROW, RSRC3);
#pragma unroll
            for (int i = 0; i < 14; ++i) issue_wo(rs_o, L_WKV + (b3 * 28 + 7 * G) * 2048, G * 112 * XROW + ct3 * 128, i);
            wait_vm<0>();
            if (l == 0) __hip_atomic_fetch_add((LAS unsigned*)L_CNT, 1u, __ATOMIC_RELAXED, __HIP_MEMORY_SCOPE_WORKGROUP);
        } else {
            const int st = w & 1, b3 = w >> 1, T = rt3 * 2 + st;
            if (__builtin_expect(!__all(fbw.x == (unsigned)tag && fbw.y == (unsigned)(tag >> 32)), 0)) {
#pragma nounroll
                for (unsigned spin = 0; spin < SPIN_CAP; ++spin) {
                    __builtin_amdgcn_s_sleep(4);
                    if (__all(__hip_atomic_load((const GAS u64*)((GAS char*)P.ws + fbo), RLX_AGENT) == tag)) break;
                }
            }
            __builtin_amdgcn_fence(__ATOMIC_ACQUIRE, "wavefront");
            if (DELAY2) __builtin_amdgcn_s_sleep(DELAY2);
            u32x4 f0[4], f2[4], f4[4], f6[4]; u32x3 t1[4], t5[4]; u32x2 t3a[4], t3b[4]; unsigned s1[4], s5[4];
            const int obase = WS_O + (T * 32 + b3 * 16) * OBLK;
            auto ldo = [&](int G, int i) __attribute__((always_inline)) {
                const int o = obase + 4 * G * OBLK;
                if (i == 0) f0[G] = __builtin_amdgcn_raw_buffer_load_b128(rs_ws, o + l * 16, 0, AUX_SC1);
                if (i == 1) t1[G] = __builtin_amdgcn_raw_buffer_load_b96(rs_ws, o + 1024 + l * 12, 0, AUX_SC1);
                if (i == 2) s1[G] = __builtin_amdgcn_raw_buffer_load_b32(rs_ws, o + OBLK + 1536 + l * 4, 0, AUX_SC1);
                if (i == 3) f2[G] = __builtin_amdgcn_raw_buffer_load_b128(rs_ws, o + OBLK + l * 16, 0, AUX_SC1);
                if (i == 4) t3a[G] = __builtin_amdgcn_raw_buffer_load_b64(rs_ws, o + OBLK + 1024 + l * 8, 0, AUX_SC1);
                if (i == 5) t3b[G] = __builtin_amdgcn_raw_buffer_load_b64(rs_ws, o + 2 * OBLK + 1024 + l * 8, 0, AUX_SC1);
                if (i == 6) f4[G] = __builtin_amdgcn_raw_buffer_load_b128(rs_ws, o + 2 * OBLK + l * 16, 0, AUX_SC1);
                if (i == 7) t5[G] = __builtin_amdgcn_raw_buffer_load_b96(rs_ws, o + 3 * OBLK + 1024 + l * 12, 0, AUX_SC1);
                if (i == 8) s5[G] = __builtin_amdgcn_raw_buffer_load_b32(rs_ws, o + 2 * OBLK + 1536 + l * 4, 0, AUX_SC1);
                if (i == 9) f6[G] = __builtin_amdgcn_raw_buffer_load_b128(rs_ws, o + 3 * OBLK + l * 16, 0, AUX_SC1);
            };
            auto ldg = [&](int G) __attribute__((always_inline)) {
#pragma unroll
                for (int i = 0; i < 10; ++i) ldo(G, i);
            };
            auto frag = [&](int G, int m) __attribute__((always_inline)) {
                return m == 0 ? f0[G] : m == 1 ? u32x4{t1[G].x, t1[G].y, t1[G].z, s1[G]} : m == 2 ? f2[G] : m == 3 ? u32x4{t3a[G].x, t3a[G].y, t3b[G].x, t3b[G].y}
                     : m == 4 ? f4[G] : m == 5 ? u32x4{t5[G].x, t5[G].y, t5[G].z, s5[G]} : f6[G];
            };
            auto emptyg = [&](int G, unsigned m) __attribute__((always_inline)) {
#pragma unroll
                for (int i = 0; i < 7; ++i) m = umax4(m, frag(G, i));
                return __any(m == EMPTY);
            };
            unsigned hint = __builtin_amdgcn_raw_buffer_load_b32(rs_ws, obase + (l & 15) * OBLK + OBLK - 4, 0, AUX_SC1);
#pragma unroll
            for (int G = 0; G < NPRE; ++G) ldg(G);
            SB();
            if (__builtin_expect(emptyg(0, hint), 0)) {
#pragma nounroll
                for (unsigned spin = 0; spin < SPIN_CAP; ++spin) {
                    __builtin_amdgcn_s_sleep(RSLEEP);
                    asm volatile("" ::: "memory");
                    hint = __builtin_amdgcn_raw_buffer_load_b32(rs_ws, obase + (l & 15) * OBLK + OBLK - 4, 0, AUX_SC1);
                    ldg(0);
                    if (!emptyg(0, hint)) break;
                }
#pragma unroll
                for (int G = 1; G < NPRE; ++G) ldg(G);
                SB();
            }
            STAMPX(12);
            STAMPY(1);
            f32x16 c0 = {}, c1 = {};
            const int q4 = (l & 15) >> 2;
            const unsigned wl = L_WKV + b3 * 57344 + (8 * hh + q4) * 128 + ((l >> 4) & 1) * 32 + (l & 3) * 8;
            const unsigned w0a = wl + ((q4 >> 1) & 1) * 64, w1a = wl + (1 - ((q4 >> 1) & 1)) * 64;
            u32x4 a0[4], a1[4];
            auto rdw = [&](int f) __attribute__((always_inline)) {
                const u32x2 x0 = trd(w0a + f * 2048), x1 = trd(w0a + f * 2048 + 512), y0 = trd(w1a + f * 2048), y1 = trd(w1a + f * 2048 + 512);
                a0[f & 3] = u32x4{x0.x, x0.y, x1.x, x1.y}; a1[f & 3] = u32x4{y0.x, y0.y, y1.x, y1.y};
            };
            rdw(0); rdw(1); rdw(2);
            SB();
#pragma unroll
            for (int G = 0; G < 4; ++G) {
                STAMPY(2 + G);
                if (G == 2) {
#pragma nounroll
                    for (unsigned spin = 0; spin < SPIN_CAP; ++spin) {
                        if (__hip_atomic_load((LAS unsigned*)L_CNT, __ATOMIC_RELAXED, __HIP_MEMORY_SCOPE_WORKGROUP) == 4u) break;
                        __builtin_amdgcn_s_sleep(1);
                    }
                    asm volatile("" ::: "memory");
                }
                unsigned mnext = 0;
#pragma unroll
                for (int m = 0; m < 7; ++m) {
                    const int f = 7 * G + m;
                    if (G == 2 && m == 0) { rdw(14); rdw(15); rdw(16); }
                    if (f + 3 < 28 && (f + 3 < 14 || G >= 2)) rdw(f + 3);
                    if (G == 0) {
                        constexpr int NR = (4 - NPRE) * 10;
#pragma unroll
                        for (int i = (NR * m) / 7; i < (NR * (m + 1)) / 7; ++i) ldo(NPRE + i / 10, i % 10);
                    }
                    SB();
                    const u32x4 bf = frag(G, m);
                    c0 = mfma32(a0[f & 3], bf, c0);
                    c1 = mfma32(a1[f & 3], bf, c1);
                    if (G < 3 && (G + 1 < NPRE || G > 0)) mnext = umax4(mnext, frag(G + 1, m));
                    SB();
                }
                if (G < 3) {
                    if (!(G + 1 < NPRE || G > 0)) {
#pragma unroll
                        for (int i = 0; i < 7; ++i) mnext = umax4(mnext, frag(G + 1, i));
                    }
                    if (__builtin_expect(__any(mnext == EMPTY), 0)) {
#pragma nounroll
                        for (unsigned spin = 0; spin < SPIN_CAP; ++spin) {
                            asm volatile("" ::: "memory");
                            ldg(G + 1);
                            if (!emptyg(G + 1, 0u)) break;
                        }
                    }
                }
            }
            STAMPY(6);
            const unsigned ra = L_XS2 + (b3 * 4 + st * 2) * 4096 + r32 * 128;
#pragma unroll
            for (int g = 0; g < 4; ++g) {
                const unsigned ch = ((2 * g + hh) ^ (r32 & 7)) * 16;
                st128(ra + ch, __builtin_bit_cast(u32x4, f32x4{c0[4 * g], c0[4 * g + 1], c0[4 * g + 2], c0[4 * g + 3]}));
                st128(ra + 4096 + ch, __builtin_bit_cast(u32x4, f32x4{c1[4 * g], c1[4 * g + 1], c1[4 * g + 2], c1[4 * g + 3]}));
            }
        }
        STAMPX(13);
        barrier();
        {
            const int row = tid >> 3, c8 = tid & 7, st = row >> 5, s = row & 31, nt = c8 >> 2, ch = 2 * (c8 & 3);
            const unsigned ra = L_XS2 + (st * 2 + nt) * 4096 + s * 128;
            const unsigned o0 = (ch ^ (s & 7)) * 16, o1 = ((ch + 1) ^ (s & 7)) * 16;
            const f32x4 a0 = __builtin_bit_cast(f32x4, ld128(ra + o0)) + __builtin_bit_cast(f32x4, ld128(ra + 16384 + o0));
            const f32x4 a1 = __builtin_bit_cast(f32x4, ld128(ra + o1)) + __builtin_bit_cast(f32x4, ld128(ra + 16384 + o1));
            const u32x4 o = {pk2(a0[0], a0[1]), pk2(a0[2], a0[3]), pk2(a1[0], a1[1]), pk2(a1[2], a1[3])};
            *(GAS u32x4*)((GAS char*)P.out + (rt3 * 64 + row) * XROW + ct3 * 128 + c8 * 16) = o;
        }
        if (bid == 0 && tid == 0) __hip_atomic_store((GAS u64*)((GAS char*)P.ws + WS_E), tag, RLX_AGENT);
    }
    STAMPX(14);
}

extern "C" void kernel_launch(void* const* d_in, const int* in_sizes, int n_in, void* d_out, int out_size, void* d_ws, size_t ws_size,
                              hipStream_t stream) {
    Params P;
    P.x = d_in[0]; P.wqkv0 = d_in[1]; P.wphi0 = d_in[2]; P.wo0 = d_in[3];
    P.wqkv1 = d_in[5]; P.wphi1 = d_in[6]; P.wo1 = d_in[7]; P.gate = d_in[8];
    P.out = d_out; P.ws = d_ws;
    hipFuncSetAttribute((const void*)hedgehog_mix_kernel, hipFuncAttributeMaxDynamicSharedMemorySize, LDS_BYTES);
    hedgehog_mix_kernel<<<256, 512, LDS_BYTES, stream>>>(P);
}
```

```cpp
#include <hip/hip_runtime.h>
#include <hip/hip_bf16.h>
#include <stdint.h>

typedef __attribute__((ext_vector_type(8))) __bf16 bf16x8;
typedef __attribute__((ext_vector_type(2))) __bf16 bf16x2;
typedef __attribute__((ext_vector_type(4))) short s16x4;
typedef __attribute__((ext_vector_type(16))) float f32x16;
typedef __attribute__((ext_vector_type(4))) float f32x4;
typedef __attribute__((ext_vector_type(4))) unsigned u32x4;
typedef __attribute__((ext_vector_type(3))) unsigned u32x3;
typedef __attribute__((ext_vector_type(2))) unsigned u32x2;
typedef unsigned long long u64;
#define LAS __attribute__((address_space(3)))
#define GAS __attribute__((address_space(1)))
#define DI __device__ __forceinline__
#define AI __attribute__((always_inline))
#define RLX_AGENT __ATOMIC_RELAXED, __HIP_MEMORY_SCOPE_AGENT

constexpr int SEQ = 2048, DM = 448, DH = 28;
constexpr int XROW = DM * 2;
constexpr int WROW = 3 * DM * 2;
constexpr int AUX_SC1 = 16, RSRC3 = 0x00020000;

constexpr int L_WKV = 0;
constexpr int L_XS0 = 57344;
constexpr int L_XS1 = L_XS0 + 32768;
constexpr int L_XS2 = L_XS1 + 32768;
constexpr int L_WPHI = 155648;
constexpr int L_GATE = L_WPHI + 2048;
constexpr int L_E = L_GATE + 1024;
constexpr int L_CNT = L_E + 256;
constexpr int L_KVT = L_WPHI + 4096;
constexpr int LDS_BYTES = 163840;

constexpr int WS_E = 0;
constexpr int WS_FA1 = 4096;
constexpr int WS_FA2 = WS_FA1 + 16384;
constexpr int WS_PART = 65536;
constexpr int PBLK = 512;
constexpr int WS_O = WS_PART + 32 * 64 * PBLK;
constexpr int OBLK = 1792;
constexpr int WS_END = WS_O + 64 * 32 * OBLK;
constexpr unsigned SPIN_CAP = 1u << 14;
constexpr unsigned EMPTY = 0xFFFFFFFFu;

constexpr int QPF = 4;
constexpr int FA2STEP = 12;
constexpr int PRT = 2;
constexpr int NPRE = 2;
constexpr int RSLEEP = 4, DELAY2 = 12;

struct Params {
    const void* x; const void* wqkv0; const void* wqkv1; const void* wphi0; const void* wphi1;
    const void* wo0; const void* wo1; const void* gate; void* out; void* ws;
};

DI unsigned pk2(float a, float b) { bf16x2 v = {(__bf16)a, (__bf16)b}; return __builtin_bit_cast(unsigned, v); }
DI f32x16 mfma32(u32x4 a, u32x4 b, f32x16 c) {
    return __builtin_amdgcn_mfma_f32_32x32x16_bf16(__builtin_bit_cast(bf16x8, a), __builtin_bit_cast(bf16x8, b), c, 0, 0, 0);
}
DI f32x4 mfma16(u32x4 a, u32x4 b, f32x4 c) {
    return __builtin_amdgcn_mfma_f32_16x16x32_bf16(__builtin_bit_cast(bf16x8, a), __builtin_bit_cast(bf16x8, b), c, 0, 0, 0);
}
DI u32x2 trd(unsigned a) { return __builtin_bit_cast(u32x2, __builtin_amdgcn_ds_read_tr16_b64_v4i16((LAS s16x4*)a)); }
DI u32x4 trfrag(unsigned a) { u32x2 lo = trd(a), hi = trd(a + 256); return u32x4{lo.x, lo.y, hi.x, hi.y}; }
DI u32x4 ld128(unsigned a) { return *(LAS const u32x4*)a; }
DI void st128(unsigned a, u32x4 v) { *(LAS u32x4*)a = v; }
DI void st64(unsigned a, u32x2 v) { *(LAS u32x2*)a = v; }
template <int OFF> DI void dma16(unsigned m0, unsigned voff, __amdgpu_buffer_rsrc_t srd, unsigned soff) {
    asm volatile("s_mov_b32 m0, %0\n\ts_nop 0\n\tbuffer_load_dwordx4 %1, %2, %3 offen offset:%4 lds"
                 :: "s"(m0), "v"(voff), "s"(srd), "s"(soff), "i"(OFF) : "memory");
}
DI void dma4(unsigned m0, unsigned voff, __amdgpu_buffer_rsrc_t srd) {
    asm volatile("s_mov_b32 m0, %0\n\ts_nop 0\n\tbuffer_load_dword %1, %2, 0 offen sc1 lds" :: "s"(m0), "v"(voff), "s"(srd) : "memory");
}
DI void touch(unsigned m0, const char* p) { asm volatile("s_mov_b32 m0, %0\n\ts_nop 0\n\tglobal_load_lds_dword %1, off" :: "s"(m0), "v"(p) : "memory"); }
DI u32x4 gld16(unsigned voff, __amdgpu_buffer_rsrc_t srd, unsigned soff) {
    u32x4 r; asm volatile("buffer_load_dwordx4 %0, %1, %2, %3 offen" : "=v"(r) : "v"(voff), "s"(srd), "s"(soff) : "memory"); return r;
}
template <int OFF = 0> DI u32x2 gld8c(unsigned voff, __amdgpu_buffer_rsrc_t srd) {
    u32x2 r; asm volatile("buffer_load_dwordx2 %0, %1, %2, 0 offen offset:%3 sc1" : "=v"(r) : "v"(voff), "s"(srd), "i"(OFF) : "memory"); return r;
}
template <int N> DI void wait_vm() { asm volatile("s_waitcnt vmcnt(%0)" :: "i"(N) : "memory"); }
DI void barrier() { asm volatile("s_waitcnt lgkmcnt(0)\n\ts_barrier" ::: "memory"); }
DI float xsum(float v) { auto r = __builtin_amdgcn_permlane32_swap(__float_as_uint(v), __float_as_uint(v), false, false); return __uint_as_float(r[0]) + __uint_as_float(r[1]); }
DI float xupper(float v) { auto r = __builtin_amdgcn_permlane32_swap(__float_as_uint(v), __float_as_uint(v), false, false); return __uint_as_float(r[1]); }

DI float max3f(float a, float b, float c) { return __builtin_fmaxf(__builtin_fmaxf(a, b), c); }
DI float min3f(float a, float b, float c) { return __builtin_fminf(__builtin_fminf(a, b), c); }
template <bool NORM, class F> DI void hedgehog(const f32x16& p, u32x4& pos0, u32x4& pos1, u32x4& neg0, u32x4& neg1, F tick) {
    constexpr float C = 1.4426950408889634f;
    float mx = __builtin_fmaxf(p[0], p[1]), mn = __builtin_fminf(p[0], p[1]);
#pragma unroll
    for (int i = 2; i < 14; i += 2) { mx = max3f(mx, p[i], p[i + 1]); mn = min3f(mn, p[i], p[i + 1]); }
    { auto r = __builtin_amdgcn_permlane32_swap(__float_as_uint(mx), __float_as_uint(mx), false, false); mx = __builtin_fmaxf(__uint_as_float(r[0]), __uint_as_float(r[1])); }
    { auto r = __builtin_amdgcn_permlane32_swap(__float_as_uint(mn), __float_as_uint(mn), false, false); mn = __builtin_fminf(__uint_as_float(r[0]), __uint_as_float(r[1])); }
    float a = -mx * C, b = mn * C;
    tick(0);
    if (!NORM) asm volatile("" : "+v"(a), "+v"(b));
    float ep[14], en[14];
    float sp = 0.f, sn = 0.f;
#pragma unroll
    for (int i = 0; i < 14; ++i) {
        ep[i] = __builtin_amdgcn_exp2f(__builtin_fmaf(p[i], C, a)); en[i] = __builtin_amdgcn_exp2f(__builtin_fmaf(p[i], -C, b));
        if (NORM) { if (i == 0) { sp = ep[0]; sn = en[0]; } else { sp += ep[i]; sn += en[i]; } }
        if (i % 3 == 2) { tick(1 + i / 3); if (!NORM) asm volatile("" : "+v"(a), "+v"(b)); }
    }
    if (NORM) {
        const float rp = __builtin_amdgcn_rcpf(xsum(sp)), rn = __builtin_amdgcn_rcpf(xsum(sn));
#pragma unroll
        for (int i = 0; i < 14; ++i) { ep[i] *= rp; en[i] *= rn; }
    }
    tick(5);
    pos0 = u32x4{pk2(ep[0], ep[1]), pk2(ep[2], ep[3]), pk2(ep[4], ep[5]), pk2(ep[6], ep[7])};
    pos1 = u32x4{pk2(ep[8], ep[9]), pk2(ep[10], ep[11]), pk2(ep[12], ep[13]), 0u};
    tick(6);
    neg0 = u32x4{pk2(en[0], en[1]), pk2(en[2], en[3]), pk2(en[4], en[5]), pk2(en[6], en[7])};
    neg1 = u32x4{pk2(en[8], en[9]), pk2(en[10], en[11]), pk2(en[12], en[13]), 0u};
}
DI u32x4 pack8(const f32x16& a, int s) { return u32x4{pk2(a[8 * s], a[8 * s + 1]), pk2(a[8 * s + 2], a[8 * s + 3]), pk2(a[8 * s + 4], a[8 * s + 5]), pk2(a[8 * s + 6], a[8 * s + 7])}; }

DI unsigned nz(unsigned v) { return v < EMPTY - 1 ? v : EMPTY - 1; }
DI unsigned umax3(unsigned a, unsigned b, unsigned c) { unsigned m = a > b ? a : b; return m > c ? m : c; }
DI unsigned umax4(unsigned m, u32x4 v) { return umax3(umax3(m, v.x, v.y), v.z, v.w); }
#define SB() __builtin_amdgcn_sched_barrier(0)

__global__ void __launch_bounds__(512, 2) hedgehog_mix_kernel(Params P) {
    const int tid = threadIdx.x, l = tid & 63;
    const int w = __builtin_amdgcn_readfirstlane(tid >> 6);
    const int bid = blockIdx.x, xcd = bid & 7, idx = bid >> 3;
    const int rb = (xcd & 1) * 4 + (idx & 3), bh = (xcd >> 1) * 8 + (idx >> 2);
    const int br = bh >> 4, hd = bh & 15;
    const bool has3 = idx < 28;
    const int rt3 = xcd * 4 + (idx & 3), ct3 = idx >> 2;
    const int r32 = l & 31, hh = l >> 5;

    const __amdgpu_buffer_rsrc_t rs_x = __builtin_amdgcn_make_buffer_rsrc((void*)P.x, 0, SEQ * XROW, RSRC3);
    const __amdgpu_buffer_rsrc_t rs_w = __builtin_amdgcn_make_buffer_rsrc((void*)(br ? P.wqkv1 : P.wqkv0), 0, DM * WROW, RSRC3);
    const __amdgpu_buffer_rsrc_t rs_ws = __builtin_amdgcn_make_buffer_rsrc(P.ws, 0, WS_END, RSRC3);

    const unsigned vw = (l >> 2) * WROW + (l & 3) * 16;
    const unsigned xrow0 = (rb * 256 + w * 32) * XROW;
    const unsigned vxe = (l >> 3) * XROW + (((l & 7) ^ (l >> 4)) * 16), vxo = (l >> 3) * XROW + (((l & 7) ^ (4 + (l >> 4))) * 16);
    const unsigned wso = (w & 3) * 16 * WROW + (1 + (w >> 2)) * XROW + hd * 56, wm0 = L_WKV + (w >> 2) * 28672 + (w & 3) * 1024;
    auto load_w = [&](int c) AI { return gld16(vw, rs_w, wso + c * 64 * WROW); };
    const unsigned wst = wm0 + l * 16;
    auto issue_x = [&](int c, int p) AI {
        const unsigned m0 = L_XS0 + (c % 3) * 32768 + w * 4096, so = xrow0 + c * 128;
        if (p == 0) dma16<0>(m0, vxe, rs_x, so);
        if (p == 1) dma16<1024>(m0, vxo, rs_x, so + 8 * XROW - 1024);
        if (p == 2) dma16<2048>(m0, vxe, rs_x, so + 16 * XROW - 2048);
        if (p == 3) dma16<3072>(m0, vxo, rs_x, so + 24 * XROW - 3072);
    };
    asm volatile("s_nop 4" ::: "memory");
    u32x4 wr[3];
    wr[0] = load_w(0);
    issue_x(0, 0); issue_x(0, 1); issue_x(0, 2); issue_x(0, 3);
    if (w == 0) {
        dma4(L_E, (l & 1) * 4, rs_ws);
    } else if (w == 1) {
        const __amdgpu_buffer_rsrc_t rs_g = __builtin_amdgcn_make_buffer_rsrc((void*)P.gate, 0, XROW, RSRC3);
        dma16<0>(L_GATE, l * 16, rs_g, 0);
    } else if (w < 4) {
        const __amdgpu_buffer_rsrc_t rs_p = __builtin_amdgcn_make_buffer_rsrc((void*)(br ? P.wphi1 : P.wphi0), 0, 16 * DH * DH * 2, RSRC3);
        dma16<0>(L_WPHI + (w - 2) * 1024, l * 16, rs_p, hd * (DH * DH * 2) + (w - 2) * 1024);
    } else {
        dma4(L_KVT + (w - 4) * 256, (l & 1) * 4, rs_ws);
    }
    __builtin_amdgcn_s_barrier();
    wr[1] = load_w(1);
    issue_x(1, 0); issue_x(1, 1); issue_x(1, 2); issue_x(1, 3);
    const int part_off = WS_PART + ((bh * 8 + w) * 8 + rb) * PBLK + l * (PBLK / 64);
    const int o_off = WS_O + ((rb * 8 + w) * 32 + bh) * OBLK;
    const u32x4 e4 = {EMPTY, EMPTY, EMPTY, EMPTY};
    { const u32x2 e2 = {EMPTY, EMPTY}; asm volatile("buffer_store_dwordx2 %0, %1, %2, 0 offen sc1" :: "v"(e2), "v"(part_off), "s"(rs_ws) : "memory"); }
    {
        const int n = w * 64 + l;
        const char* pa;
        if (n < 320) { const int id = (bh & 7) * 320 + n, line = id >> 1, row = line / 5; pa = (const char*)P.x + (rb * 256 + row) * XROW + (2 + line - row * 5) * 128 + (id & 1) * 64; }
        else { const int id = idx * 140 + (n < 460 ? n - 320 : 0), part = id >= 2240, ip = id - part * 2240, r = ip / 7;
               pa = (const char*)(br ? P.wqkv1 : P.wqkv0) + (128 + r) * WROW + (1 + part) * XROW + ((bh >> 3) & 1) * 448 + (ip - r * 7) * 64; }
        touch(L_KVT + 2048 + w * 256, pa);
    }
    const unsigned trl = (8 * hh + ((l & 15) >> 2)) * 64 + ((l >> 4) & 1) * 32 + (l & 3) * 8;
    unsigned xa[4];
#pragma unroll
    for (int t = 0; t < 4; ++t) xa[t] = L_XS0 + w * 4096 + r32 * 128 + (((hh ^ ((r32 >> 1) & 7)) * 16) ^ (32 * t));
    u32x4 xf[28], wp0, wp1;
    u64 tag = 0;
    f32x16 ak = {}, av = {};
    unsigned a_phi = L_XS0 + (w * 32 + r32) * 128 + (((2 * hh) ^ ((r32 & 7) ^ (((r32 >> 3) & 1) << 2))) << 4);
    unsigned a_v = L_XS1 + w * 4096 + r32 * 64 + 8 * hh + (((0x78 >> (2 * ((r32 >> 2) & 3))) & 3) << 4);
    unsigned a_kva, a_kvb, a_kvv, a_kvt, a_num;
    {
        const int ft = w >> 1, dt = w & 1, i16 = l & 15, q = i16 >> 2, p4 = i16 & 3, g = l >> 4, c16 = 2 * ft + (p4 >> 1), d = 16 * dt + i16;
        a_kva = L_XS0 + (8 * g + q) * 128 + ((c16 ^ (q ^ ((g & 1) << 2))) * 16) + 8 * (p4 & 1);
        a_kvb = L_XS0 + (8 * g + 4 + q) * 128 + ((c16 ^ ((4 + q) ^ ((g & 1) << 2))) * 16) + 8 * (p4 & 1);
        a_kvv = L_XS1 + d * 64 + ((g ^ ((0x78 >> (2 * ((d >> 2) & 3))) & 3)) * 16);
        a_kvt = L_KVT + d * 128 + (((2 * ft + (l >> 5)) ^ (d & 7)) * 16) + ((l >> 4) & 1) * 8;
        const int rg = (r32 & 3) + 4 * (r32 >> 3), dn = rg < 14 ? 14 * ((r32 >> 2) & 1) + rg : 14 + rg;
        a_num = L_KVT + dn * 128 + (((2 * hh) ^ (dn & 7)) << 4);
    }
    asm volatile("" : "+v"(a_phi), "+v"(a_v), "+v"(a_kva), "+v"(a_kvb), "+v"(a_kvv), "+v"(a_kvt), "+v"(a_num));
    const GAS u64* fap = (const GAS u64*)((GAS char*)P.ws + WS_FA1) + (bh * 8 + w) * 8 + (l & 7);
    u64 faw = 0;
    asm volatile("s_waitcnt vmcnt(7)" : "+v"(wr[0]) :: "memory");
    st128(wst, wr[0]);
    if (tid == 0) *(LAS unsigned*)L_CNT = 0;
    barrier();
#pragma unroll
    for (int c = 0; c < 7; ++c) {
        if (c <= 4) issue_x(c + 2, 0);
        if (c == 5) faw = __hip_atomic_load(fap, RLX_AGENT);
        if (c == 6) dma16<0>(L_XS2 + w * 4096, vw, rs_w, (w < 7 ? w : 6) * 64 * WROW + hd * 56);
#pragma unroll
        for (int t = 0; t < 4; ++t) xf[4 * c + t] = ld128(xa[t] + (c % 3) * 32768);
        u32x4 fk[2], fv[2];
        fk[0] = trfrag(L_WKV + trl + 4 * c * 1024); fv[0] = trfrag(L_WKV + 28672 + trl + 4 * c * 1024);
        if (c == 0) tag = *(LAS const u64*)L_E + 1;
        if (c == 6) { wp0 = ld128(L_KVT + l * 16); wp1 = ld128(L_KVT + 1024 + l * 16); }
        SB();
#pragma unroll
        for (int t = 0; t < 4; ++t) {
            const int ks = 4 * c + t;
            if (t < 3) { fk[(t + 1) & 1] = trfrag(L_WKV + trl + (ks + 1) * 1024); fv[(t + 1) & 1] = trfrag(L_WKV + 28672 + trl + (ks + 1) * 1024); }
            SB();
            ak = mfma32(fk[t & 1], xf[ks], ak);
            av = mfma32(xf[ks], fv[t & 1], av);
            SB();
            if (c == 6 && t == 1) asm volatile("" : "+v"(wp0), "+v"(wp1));
            if (c <= 4) { if (t < 3) issue_x(c + 2, t + 1); else wr[(c + 2) % 3] = load_w(c + 2); }
            if (c == 6 && t == 3) {
                const int n = w * 64 + l, hl = idx * 392 + (n < 392 ? n : 0);
                const char* pa = (hl < 6272 ? (const char*)P.wo0 : (const char*)P.wo1 - 6272 * 64) + hl * 64;
                touch(L_KVT + 2048 + w * 256, pa);
            }
            if (c == 6 && t < 3) {
                const unsigned so = (w < 7 ? w : 6) * 64 * WROW + hd * 56 + (t + 1) * (16 * WROW - 1024);
                if (t == 0) dma16<1024>(L_XS2 + w * 4096, vw, rs_w, so); else if (t == 1) dma16<2048>(L_XS2 + w * 4096, vw, rs_w, so); else dma16<3072>(L_XS2 + w * 4096, vw, rs_w, so);
            }
            SB();
        }
        if (c == 0) {
            const int ln = (tid >> 2) & 63, i = ln & 31, j = 2 * (tid & 3);
            const int rg = (i & 3) + 4 * (i >> 3), f = 14 * ((i >> 2) & 1) + rg;
            const int d = 16 * (tid >> 8) + 8 * (j >> 2) + 4 * (ln >> 5) + (j & 3);
            const bool ok = rg < 14 && d < DH;
            const unsigned a = L_WPHI + (ok ? d * DH + f : 0) * 2;
            const unsigned lo = *(LAS const unsigned short*)a, hi = *(LAS const unsigned short*)(a + 2 * DH);
            *(LAS unsigned*)(L_KVT + tid * 4) = ok ? lo | hi << 16 : 0u;
            SB();
        }
        if (c < 6) {
            if (c == 0) asm volatile("s_waitcnt vmcnt(6)" : "+v"(wr[(c + 1) % 3]) :: "memory");
            else if (c == 1) asm volatile("s_waitcnt vmcnt(6)" : "+v"(wr[(c + 1) % 3]) :: "memory");
            else if (c == 5) asm volatile("s_waitcnt vmcnt(0)" : "+v"(wr[(c + 1) % 3]) :: "memory");
            else asm volatile("s_waitcnt vmcnt(5)" : "+v"(wr[(c + 1) % 3]) :: "memory");
            st128(wst + (c + 1) * 4096, wr[(c + 1) % 3]);
            if (c == 0 && l == 0) __hip_atomic_store((GAS u64*)((GAS char*)P.ws + WS_FA1) + (bh * 8 + w) * 8 + rb, tag, RLX_AGENT);
            if (c == 5) {
#pragma nounroll
                for (unsigned spin = 0; spin < SPIN_CAP && !__all(faw == tag); ++spin) { __builtin_amdgcn_s_sleep(2); faw = __hip_atomic_load(fap, RLX_AGENT); }
                __builtin_amdgcn_fence(__ATOMIC_ACQUIRE, "wavefront");
            }
            barrier();
        }
    }
    {
        f32x16 p = {};
        p = mfma32(wp0, pack8(ak, 0), p);
        p = mfma32(wp1, pack8(ak, 1), p);
        const bool one = r32 == 28;
#pragma unroll
        for (int g = 0; g < 4; ++g) {
            u32x2 v2 = {pk2(av[4 * g], av[4 * g + 1]), pk2(av[4 * g + 2], av[4 * g + 3])};
            if (one) v2 = u32x2{0x3F803F80u, 0x3F803F80u};
            st64(a_v ^ (g * 16), v2);
        }
        u32x4 pos0, pos1, neg0, neg1;
        hedgehog<true>(p, pos0, pos1, neg0, neg1, [](int) AI {});
        st128(a_phi, pos0); st128(a_phi ^ 16, pos1); st128(a_phi ^ 64, neg0); st128(a_phi ^ 80, neg1);
    }
    {
        asm volatile("buffer_store_dwordx4 %0, %1, %2, 0 offen sc1\n\ts_nop 1" :: "v"(e4), "v"(o_off + l * 16), "s"(rs_ws) : "memory");
        const u32x3 e3 = {EMPTY, EMPTY, EMPTY};
        asm volatile("buffer_store_dwordx3 %0, %1, %2, 0 offen sc1\n\ts_nop 1" :: "v"(e3), "v"(o_off + 1024 + l * 12), "s"(rs_ws) : "memory");
    }
    barrier();
    {
        u32x2 lo[8], hi[8]; u32x4 bv[8];
#pragma unroll
        for (int t = 0; t < 8; ++t) { lo[t] = trd(a_kva + t * 4096); hi[t] = trd(a_kvb + t * 4096); bv[t] = ld128(a_kvv + t * 4096); }
        const unsigned short gv = *(LAS const unsigned short*)(L_GATE + ((w < 7 ? w : 6) * 64 + l) * 2);
        SB();
        f32x4 kv = {}, kw = {};
#pragma unroll
        for (int t = 0; t < 8; t += 2) {
            kv = mfma16(u32x4{lo[t].x, lo[t].y, hi[t].x, hi[t].y}, bv[t], kv);
            kw = mfma16(u32x4{lo[t + 1].x, lo[t + 1].y, hi[t + 1].x, hi[t + 1].y}, bv[t + 1], kw);
        }
        kv += kw;
        __builtin_amdgcn_raw_buffer_store_b64(u32x2{nz(pk2(kv[0], kv[1])), nz(pk2(kv[2], kv[3]))}, rs_ws, part_off, 0, AUX_SC1);
        wait_vm<3>();
        *(LAS unsigned short*)(L_XS2 + w * 4096 + l * 64 + 56) = gv;
    }
    barrier();
    f32x16 aq = {};
    const int jr = l >> 3;
    const unsigned vch = ((l & 7) ^ (((jr >> 1) & 1) << 2)) * 16;
    auto issue_wo = [&](__amdgpu_buffer_rsrc_t rs, unsigned m0, unsigned so, int i) AI {
        const int m = i >> 1, hp = i & 1, n56 = 8 * m + (m == 5 ? (jr + 2) & 7 : jr), h4 = n56 / 14;
        dma16<0>(m0 + m * 2048 + hp * 1024, (n56 + 14 * (h4 + hp)) * XROW + vch, rs, so);
    };
    {
        const __amdgpu_buffer_rsrc_t rs_o = __builtin_amdgcn_make_buffer_rsrc((void*)(w >= 4 ? P.wo1 : P.wo0), 0, has3 ? DM * XROW : 0, RSRC3);
        const unsigned wo_m0 = L_WKV + ((w >> 2) * 28 + 7 * ((w >> 1) & 1)) * 2048, wo_so = ((w >> 1) & 1) * 112 * XROW + ct3 * 128;
        unsigned trq = L_XS2 + trl;
        asm volatile("" : "+v"(trq));
        u32x4 fq[QPF + 1];
#pragma unroll
        for (int i = 0; i < QPF; ++i) fq[i] = trfrag(trq + i * 1024);
#pragma unroll
        for (int ks = 0; ks < 28; ++ks) {
            if (ks + QPF < 28) fq[(ks + QPF) % (QPF + 1)] = trfrag(trq + (ks + QPF) * 1024);
            if ((ks & 3) == 0) { if (w & 1) issue_wo(rs_o, wo_m0, wo_so, 7 + (ks >> 2)); else issue_wo(rs_o, wo_m0, wo_so, ks >> 2); }
            if (ks == FA2STEP) {
                wait_vm<1 + FA2STEP / 4 + 1>();
                if (l == 0) __hip_atomic_store((GAS u64*)((GAS char*)P.ws + WS_FA2) + (rb * 8 + w) * 32 + bh, tag, RLX_AGENT);
            }
            SB();
            aq = mfma32(fq[ks % (QPF + 1)], xf[ks], aq);
            SB();
        }
    }
    const int pbase = WS_PART + (bh * 8 + w) * 8 * PBLK + l * (PBLK / 64);
    u32x2 pr[8];
    u32x2 fbw;
    const unsigned fbo = WS_FA2 + ((rt3 * 2 + (w & 1)) * 32 + ((w >> 1) & 1) * 16 + (l & 15)) * 8;
    u32x4 qp0, qp1, qn0, qn1;
    float gsc;
    {
        f32x16 p = {};
        p = mfma32(wp0, pack8(aq, 0), p);
        p = mfma32(wp1, pack8(aq, 1), p);
        const float zl = xupper(aq[12]);
        gsc = __builtin_amdgcn_rcpf(1.f + __builtin_amdgcn_exp2f((br ? zl : -zl) * 1.4426950408889634f));
        hedgehog<false>(p, qp0, qp1, qn0, qn1, [&](int i) AI {
            SB();
            if (i == PRT) {
                pr[0] = gld8c<0>(pbase, rs_ws); pr[1] = gld8c<PBLK>(pbase, rs_ws); pr[2] = gld8c<2 * PBLK>(pbase, rs_ws); pr[3] = gld8c<3 * PBLK>(pbase, rs_ws);
                pr[4] = gld8c<4 * PBLK>(pbase, rs_ws); pr[5] = gld8c<5 * PBLK>(pbase, rs_ws); pr[6] = gld8c<6 * PBLK>(pbase, rs_ws); pr[7] = gld8c<7 * PBLK>(pbase, rs_ws);
                fbw = gld8c(fbo, rs_ws);
            }
            SB();
        });
        asm volatile("" : "+v"(qp0), "+v"(qp1), "+v"(qn0), "+v"(qn1), "+v"(gsc));
    }
    SB();
    {
        asm volatile("s_waitcnt vmcnt(0)" : "+v"(pr[0]), "+v"(pr[1]), "+v"(pr[2]), "+v"(pr[3]), "+v"(pr[4]), "+v"(pr[5]), "+v"(pr[6]), "+v"(pr[7]), "+v"(fbw) :: "memory");
        auto empty8 = [&]() AI {
            unsigned m = 0;
#pragma unroll
            for (int r = 0; r < 8; ++r) m = umax3(m, pr[r].x, pr[r].y);
            return __any(m == EMPTY);
        };
        if (__builtin_expect(empty8(), 0)) {
#pragma nounroll
            for (unsigned spin = 0; spin < SPIN_CAP; ++spin) {
                __builtin_amdgcn_s_sleep(RSLEEP);
                asm volatile("" ::: "memory");
#pragma unroll
                for (int r = 0; r < 8; ++r) pr[r] = __builtin_amdgcn_raw_buffer_load_b64(rs_ws, pbase + r * PBLK, 0, AUX_SC1);
                if (!empty8()) break;
            }
        }
        f32x4 s = {};
#pragma unroll
        for (int r = 0; r < 8; ++r)
            s += f32x4{__uint_as_float(pr[r].x << 16), __uint_as_float(pr[r].x & 0xFFFF0000u), __uint_as_float(pr[r].y << 16), __uint_as_float(pr[r].y & 0xFFFF0000u)};
        const bool ones = (w & 1) && (l & 15) == 13;
        st64(a_kvt, ones ? u32x2{0x3F803F80u, 0x3F803F80u} : u32x2{pk2(s[0], s[1]), pk2(s[2], s[3])});
    }
    barrier();
    {
        const u32x4 a0 = ld128(a_num), a2 = ld128(a_num ^ 64), a1 = ld128(a_num ^ 16), a3 = ld128(a_num ^ 80);
        SB();
        f32x16 np = {}, nn = {};
        np = mfma32(a0, qp0, np);
        nn = mfma32(a2, qn0, nn);
        np = mfma32(a1, qp1, np);
        nn = mfma32(a3, qn1, nn);
        const float zp = np[15], zn = nn[15];
        const float sc = gsc * __builtin_amdgcn_rcpf(__builtin_fmaf(np[14], zn, __builtin_fmaf(nn[14], zp, 1e-6f * zp * zn)));
        const float fp = zn * sc, fn = zp * sc;
        unsigned o[7];
#pragma unroll
        for (int i = 0; i < 7; ++i) o[i] = nz(pk2(__builtin_fmaf(nn[2 * i], fn, np[2 * i] * fp), __builtin_fmaf(nn[2 * i + 1], fn, np[2 * i + 1] * fp)));
        const int hq = hd & 3;
        if (hq == 0) {
            __builtin_amdgcn_raw_buffer_store_b128(u32x4{o[0], o[1], o[2], o[3]}, rs_ws, o_off + l * 16, 0, AUX_SC1);
            __builtin_amdgcn_raw_buffer_store_b96(u32x3{o[4], o[5], o[6]}, rs_ws, o_off + 1024 + l * 12, 0, AUX_SC1);
        } else if (hq == 3) {
            __builtin_amdgcn_raw_buffer_store_b128(u32x4{o[3], o[4], o[5], o[6]}, rs_ws, o_off + l * 16, 0, AUX_SC1);
            __builtin_amdgcn_raw_buffer_store_b96(u32x3{o[0], o[1], o[2]}, rs_ws, o_off + 1024 + l * 12, 0, AUX_SC1);
        } else {
            const bool h1 = hq == 1;
            __builtin_amdgcn_raw_buffer_store_b128(h1 ? u32x4{o[1], o[2], o[3], o[4]} : u32x4{o[2], o[3], o[4], o[5]}, rs_ws, o_off + l * 16, 0, AUX_SC1);
            __builtin_amdgcn_raw_buffer_store_b64(h1 ? u32x2{o[5], o[6]} : u32x2{o[0], o[1]}, rs_ws, o_off + 1024 + l * 8, 0, AUX_SC1);
            __builtin_amdgcn_raw_buffer_store_b32(h1 ? o[0] : o[6], rs_ws, o_off + 1536 + l * 4, 0, AUX_SC1);
        }
    }
    if (has3) {
        if (w >= 4) {
            const int b3 = (w >> 1) & 1, G = 2 + (w & 1);
            const __amdgpu_buffer_rsrc_t rs_o = __builtin_amdgcn_make_buffer_rsrc((void*)(b3 ? P.wo1 : P.wo0), 0, DM * XROW, RSRC3);
#pragma unroll
            for (int i = 0; i < 14; ++i) issue_wo(rs_o, L_WKV + (b3 * 28 + 7 * G) * 2048, G * 112 * XROW + ct3 * 128, i);
            wait_vm<0>();
            if (l == 0) __hip_atomic_fetch_add((LAS unsigned*)L_CNT, 1u, __ATOMIC_RELAXED, __HIP_MEMORY_SCOPE_WORKGROUP);
        } else {
            const int st = w & 1, b3 = w >> 1, T = rt3 * 2 + st;
            if (__builtin_expect(!__all(fbw.x == (unsigned)tag && fbw.y == (unsigned)(tag >> 32)), 0)) {
#pragma nounroll
                for (unsigned spin = 0; spin < SPIN_CAP; ++spin) {
                    __builtin_amdgcn_s_sleep(4);
                    if (__all(__hip_atomic_load((const GAS u64*)((GAS char*)P.ws + fbo), RLX_AGENT) == tag)) break;
                }
            }
            __builtin_amdgcn_fence(__ATOMIC_ACQUIRE, "wavefront");
            if (DELAY2) __builtin_amdgcn_s_sleep(DELAY2);
            u32x4 f0[4], f2[4], f4[4], f6[4]; u32x3 t1[4], t5[4]; u32x2 t3a[4], t3b[4]; unsigned s1[4], s5[4];
            const int obase = WS_O + (T * 32 + b3 * 16) * OBLK;
            const int l16 = l * 16, l12 = l * 12, l8 = l * 8, l4 = l * 4;
            auto ldo = [&](int G, int i) AI {
                const int o = obase + 4 * G * OBLK;
                if (i == 0) f0[G] = __builtin_amdgcn_raw_buffer_load_b128(rs_ws, l16, o, AUX_SC1);
                if (i == 1) t1[G] = __builtin_amdgcn_raw_buffer_load_b96(rs_ws, l12, o + 1024, AUX_SC1);
                if (i == 2) s1[G] = __builtin_amdgcn_raw_buffer_load_b32(rs_ws, l4, o + OBLK + 1536, AUX_SC1);
                if (i == 3) f2[G] = __builtin_amdgcn_raw_buffer_load_b128(rs_ws, l16, o + OBLK, AUX_SC1);
                if (i == 4) t3a[G] = __builtin_amdgcn_raw_buffer_load_b64(rs_ws, l8, o + OBLK + 1024, AUX_SC1);
                if (i == 5) t3b[G] = __builtin_amdgcn_raw_buffer_load_b64(rs_ws, l8, o + 2 * OBLK + 1024, AUX_SC1);
                if (i == 6) f4[G] = __builtin_amdgcn_raw_buffer_load_b128(rs_ws, l16, o + 2 * OBLK, AUX_SC1);
                if (i == 7) t5[G] = __builtin_amdgcn_raw_buffer_load_b96(rs_ws, l12, o + 3 * OBLK + 1024, AUX_SC1);
                if (i == 8) s5[G] = __builtin_amdgcn_raw_buffer_load_b32(rs_ws, l4, o + 2 * OBLK + 1536, AUX_SC1);
                if (i == 9) f6[G] = __builtin_amdgcn_raw_buffer_load_b128(rs_ws, l16, o + 3 * OBLK, AUX_SC1);
            };
            auto ldg = [&](int G) AI {
#pragma unroll
                for (int i = 0; i < 10; ++i) ldo(G, i);
            };
            auto frag = [&](int G, int m) AI {
                return m == 0 ? f0[G] : m == 1 ? u32x4{t1[G].x, t1[G].y, t1[G].z, s1[G]} : m == 2 ? f2[G] : m == 3 ? u32x4{t3a[G].x, t3a[G].y, t3b[G].x, t3b[G].y}
                     : m == 4 ? f4[G] : m == 5 ? u32x4{t5[G].x, t5[G].y, t5[G].z, s5[G]} : f6[G];
            };
            auto emptyg = [&](int G, unsigned m) AI {
#pragma unroll
                for (int i = 0; i < 7; ++i) m = umax4(m, frag(G, i));
                return __any(m == EMPTY);
            };
            unsigned hint = __builtin_amdgcn_raw_buffer_load_b32(rs_ws, obase + (l & 15) * OBLK + OBLK - 4, 0, AUX_SC1);
#pragma unroll
            for (int G = 0; G < NPRE; ++G) ldg(G);
            SB();
            if (__builtin_expect(emptyg(0, hint), 0)) {
#pragma nounroll
                for (unsigned spin = 0; spin < SPIN_CAP; ++spin) {
                    __builtin_amdgcn_s_sleep(RSLEEP);
                    asm volatile("" ::: "memory");
                    hint = __builtin_amdgcn_raw_buffer_load_b32(rs_ws, obase + (l & 15) * OBLK + OBLK - 4, 0, AUX_SC1);
                    ldg(0);
                    if (!emptyg(0, hint)) break;
                }
#pragma unroll
                for (int G = 1; G < NPRE; ++G) ldg(G);
                SB();
            }
            f32x16 c0 = {}, c1 = {};
            const int q4 = (l & 15) >> 2;
            const unsigned wl = L_WKV + b3 * 57344 + (8 * hh + q4) * 128 + ((l >> 4) & 1) * 32 + (l & 3) * 8;
            const unsigned w0a = wl + ((q4 >> 1) & 1) * 64, w1a = wl + (1 - ((q4 >> 1) & 1)) * 64;
            u32x4 a0[4], a1[4];
            auto rdw = [&](int f) AI {
                const u32x2 x0 = trd(w0a + f * 2048), x1 = trd(w0a + f * 2048 + 512), y0 = trd(w1a + f * 2048), y1 = trd(w1a + f * 2048 + 512);
                a0[f & 3] = u32x4{x0.x, x0.y, x1.x, x1.y}; a1[f & 3] = u32x4{y0.x, y0.y, y1.x, y1.y};
            };
            rdw(0); rdw(1); rdw(2);
            SB();
#pragma unroll
            for (int G = 0; G < 4; ++G) {
                if (G == 2) {
#pragma nounroll
                    for (unsigned spin = 0; spin < SPIN_CAP; ++spin) {
                        if (__hip_atomic_load((LAS unsigned*)L_CNT, __ATOMIC_RELAXED, __HIP_MEMORY_SCOPE_WORKGROUP) == 4u) break;
                        __builtin_amdgcn_s_sleep(1);
                    }
                    asm volatile("" ::: "memory");
                }
                unsigned mnext = 0;
#pragma unroll
                for (int m = 0; m < 7; ++m) {
                    const int f = 7 * G + m;
                    if (G == 2 && m == 0) { rdw(14); rdw(15); rdw(16); }
                    if (f + 3 < 28 && (f + 3 < 14 || G >= 2)) rdw(f + 3);
                    if (G == 0) {
                        constexpr int NR = (4 - NPRE) * 10;
#pragma unroll
                        for (int i = (NR * m) / 7; i < (NR * (m + 1)) / 7; ++i) ldo(NPRE + i / 10, i % 10);
                    }
                    SB();
                    const u32x4 bf = frag(G, m);
                    c0 = mfma32(a0[f & 3], bf, c0);
                    c1 = mfma32(a1[f & 3], bf, c1);
                    if (G < 3 && (G + 1 < NPRE || G > 0)) mnext = umax4(mnext, frag(G + 1, m));
                    SB();
                }
                if (G < 3) {
                    if (!(G + 1 < NPRE || G > 0)) {
#pragma unroll
                        for (int i = 0; i < 7; ++i) mnext = umax4(mnext, frag(G + 1, i));
                    }
                    if (__builtin_expect(__any(mnext == EMPTY), 0)) {
#pragma nounroll
                        for (unsigned spin = 0; spin < SPIN_CAP; ++spin) {
                            asm volatile("" ::: "memory");
                            ldg(G + 1);
                            if (!emptyg(G + 1, 0u)) break;
                        }
                    }
                }
            }
            const unsigned ra = L_XS2 + (b3 * 4 + st * 2) * 4096 + r32 * 128;
#pragma unroll
            for (int g = 0; g < 4; ++g) {
                const unsigned ch = ((2 * g + hh) ^ (r32 & 7)) * 16;
                st128(ra + ch, __builtin_bit_cast(u32x4, f32x4{c0[4 * g], c0[4 * g + 1], c0[4 * g + 2], c0[4 * g + 3]}));
                st128(ra + 4096 + ch, __builtin_bit_cast(u32x4, f32x4{c1[4 * g], c1[4 * g + 1], c1[4 * g + 2], c1[4 * g + 3]}));
            }
        }
        barrier();
        {
            const int row = tid >> 3, c8 = tid & 7, st = row >> 5, s = row & 31, nt = c8 >> 2, ch = 2 * (c8 & 3);
            const unsigned ra = L_XS2 + (st * 2 + nt) * 4096 + s * 128;
            const unsigned o0 = (ch ^ (s & 7)) * 16, o1 = ((ch + 1) ^ (s & 7)) * 16;
            const f32x4 a0 = __builtin_bit_cast(f32x4, ld128(ra + o0)) + __builtin_bit_cast(f32x4, ld128(ra + 16384 + o0));
            const f32x4 a1 = __builtin_bit_cast(f32x4, ld128(ra + o1)) + __builtin_bit_cast(f32x4, ld128(ra + 16384 + o1));
            const u32x4 o = {pk2(a0[0], a0[1]), pk2(a0[2], a0[3]), pk2(a1[0], a1[1]), pk2(a1[2], a1[3])};
            const __amdgpu_buffer_rsrc_t rs_out = __builtin_amdgcn_make_buffer_rsrc(P.out, 0, SEQ * XROW, RSRC3);
            __builtin_amdgcn_raw_buffer_store_b128(o, rs_out, (rt3 * 64 + row) * XROW + ct3 * 128 + c8 * 16, 0, 0);
        }
        if (bid == 0 && tid == 0) __hip_atomic_store((GAS u64*)((GAS char*)P.ws + WS_E), tag, RLX_AGENT);
    }
}

extern "C" void kernel_launch(void* const* d_in, const int* in_sizes, int n_in, void* d_out, int out_size, void* d_ws, size_t ws_size,
                              hipStream_t stream) {
    Params P;
    P.x = d_in[0]; P.wqkv0 = d_in[1]; P.wphi0 = d_in[2]; P.wo0 = d_in[3];
    P.wqkv1 = d_in[5]; P.wphi1 = d_in[6]; P.wo1 = d_in[7]; P.gate = d_in[8];
    P.out = d_out; P.ws = d_ws;
    (void)hipFuncSetAttribute((const void*)hedgehog_mix_kernel, hipFuncAttributeMaxDynamicSharedMemorySize, LDS_BYTES);
    hedgehog_mix_kernel<<<256, 512, LDS_BYTES, stream>>>(P);
}
```
